```python
import torch

def _softmax(x, axis=-1):
    return torch.softmax(x, dim=axis)
def _gelu(x):
    return torch.nn.functional.gelu(x, approximate="tanh")
def _silu(x):
    return torch.nn.functional.silu(x)
def _sigmoid(x):
    return torch.sigmoid(x)
def _layernorm(x, axis=-1, eps=1e-5):
    m = x.mean(dim=axis, keepdim=True)
    v = x.var(dim=axis, keepdim=True, unbiased=False)
    return (x - m) / torch.sqrt(v + eps)
def _scatter_add(tgt, idx, src, axis=0):
    return tgt.clone().index_add_(axis, idx, src)
def _segment_sum(src, seg, n):
    out = torch.zeros((n,) + src.shape[1:], dtype=src.dtype)
    return out.index_add_(0, seg, src)
def _pad(x, pw):
    flat = tuple(v for pair in reversed(pw) for v in pair)
    return torch.nn.functional.pad(x, flat)

kernel_name = 'standalone__gqa_block_cached__decode'

def setup_inputs():

    _w = (lambda a: (lambda t: t.to(torch.bfloat16) if t.is_floating_point() else t)(torch.as_tensor(a)))
    B, Sq, D = 1, 1, 512
    Hq, Hkv, Sk, Dh = 32, 8, 65536, 128
    assert Hq % Hkv == 0, (Hq, Hkv)
    return {
        "x": _w((lambda *s: torch.randn(*s, dtype=torch.bfloat16) * 0.8)(B, Sq, D)),
        "k_cache": _w((lambda *s: torch.randn(*s, dtype=torch.bfloat16) * 0.8)(B, Hkv, Sk, Dh)),
        "v_cache": _w((lambda *s: torch.randn(*s, dtype=torch.bfloat16) * 0.05)(B, Hkv, Sk, Dh)),
        "wq": _w((lambda *s: torch.randn(*s, dtype=torch.bfloat16) * 0.8)(D, Hq * Dh) * (D ** -0.5)),
        "wo": _w((lambda *s: torch.randn(*s, dtype=torch.bfloat16) * 0.05)(Hq * Dh, D) * ((Hq * Dh) ** -0.5)),
    }

def reference(x, k_cache, v_cache, wq, wo):
    B, Sq, D = x.shape
    Hq, Dh = 32, 128
    Hkv = k_cache.shape[1]
    rep = Hq // Hkv
    q = (lambda x, *d: x.permute(*d))((x @ wq).reshape(B, Sq, Hq, Dh), 0, 2, 1, 3)
    k = (lambda x, n, axis: torch.repeat_interleave(x, n, dim=axis))(k_cache, rep, axis=1)
    v = (lambda x, n, axis: torch.repeat_interleave(x, n, dim=axis))(v_cache, rep, axis=1)
    scores = torch.einsum("bhqd,bhkd->bhqk", q, k) * (Dh ** -0.5)
    p = _softmax(scores, axis=-1)
    o = torch.einsum("bhqk,bhkd->bhqd", p, v)
    return (lambda x, *d: x.permute(*d))(o, 0, 2, 1, 3).reshape(B, Sq, Hq * Dh) @ wo
```

```cpp
#include <hip/hip_runtime.h>
#include <hip/hip_bf16.h>
#include <stdint.h>

typedef unsigned short u16;
typedef unsigned int u32;
typedef unsigned long long u64;
typedef u32 v4u __attribute__((ext_vector_type(4)));
typedef float f32x4 __attribute__((ext_vector_type(4)));
typedef float f32x2 __attribute__((ext_vector_type(2)));
typedef __bf16 bf16x8 __attribute__((ext_vector_type(8)));
typedef __bf16 bf16x2 __attribute__((ext_vector_type(2)));
#define GAS __attribute__((address_space(1)))

#define KN_MAP 1
#define KN_ORD 0
#define KN_L 4

constexpr int DM = 512, HQ = 32, HKV = 8, SK = 65536, DH = 128;
constexpr int NWG = 256, NW = 8, NTHR = (NW + 1) * 64;
constexpr int NBLK = SK / 32 / 32 / NW;
constexpr float C2 = 0.088388347648318440f * 1.4426950408889634f;

constexpr int WS_EPOCH = 0;
constexpr int WS_QX = 4096;
constexpr int WS_ML = 32768;
constexpr int WS_P1 = 65536;
constexpr int WS_P2 = WS_P1 + (1 << 20);
constexpr int WS_END = WS_P2 + (1 << 20);

constexpr int PROW = 132;
constexpr int L_PART = 0;
constexpr int L_ML = L_PART + NW * 4 * PROW;
constexpr int L_QRED = L_ML + NW * 4 * 2;
constexpr int L_QF = L_QRED + NW * 16;
constexpr int L_TAG = L_QF + 256;
constexpr int L_END = L_TAG + 4;

__device__ __forceinline__ v4u ld_nt(const void* p) { return __builtin_nontemporal_load((const GAS v4u*)p); }
__device__ __forceinline__ f32x4 mfma16(v4u a, v4u b, f32x4 c) {
  return __builtin_amdgcn_mfma_f32_16x16x32_bf16(__builtin_bit_cast(bf16x8, a), __builtin_bit_cast(bf16x8, b), c, 0, 0, 0);
}
__device__ __forceinline__ u32 pack_bf16(float lo, float hi) {
  bf16x2 r; r.x = (__bf16)lo; r.y = (__bf16)hi; return __builtin_bit_cast(u32, r);
}
template <int CTRL> __device__ __forceinline__ float dppf(float x) {
  return __builtin_bit_cast(float, __builtin_amdgcn_mov_dpp(__builtin_bit_cast(int, x), CTRL, 0xf, 0xf, true));
}
template <int CTRL> __device__ __forceinline__ u32 dppu(u32 x) { return (u32)__builtin_amdgcn_mov_dpp((int)x, CTRL, 0xf, 0xf, true); }
template <int BANK> __device__ __forceinline__ float bank_put(float old, float src) {
  return __builtin_bit_cast(float, __builtin_amdgcn_update_dpp(__builtin_bit_cast(int, old), __builtin_bit_cast(int, src), 0xE4, 0xf, 1 << BANK, false));
}
constexpr int ROR4 = 0x124, ROR8 = 0x128, ROR12 = 0x12C;
constexpr int XOR1 = 0xB1, XOR2 = 0x4E;
__device__ __forceinline__ float xrow_max(float x) {
  auto s = __builtin_amdgcn_permlane16_swap(__float_as_uint(x), __float_as_uint(x), false, false);
  x = fmaxf(__uint_as_float(s[0]), __uint_as_float(s[1]));
  auto t = __builtin_amdgcn_permlane32_swap(__float_as_uint(x), __float_as_uint(x), false, false);
  return fmaxf(__uint_as_float(t[0]), __uint_as_float(t[1]));
}
__device__ __forceinline__ float xrow_sum(float x) {
  auto s = __builtin_amdgcn_permlane16_swap(__float_as_uint(x), __float_as_uint(x), false, false);
  x = __uint_as_float(s[0]) + __uint_as_float(s[1]);
  auto t = __builtin_amdgcn_permlane32_swap(__float_as_uint(x), __float_as_uint(x), false, false);
  return __uint_as_float(t[0]) + __uint_as_float(t[1]);
}
__device__ __forceinline__ float e2(float x) { return __builtin_amdgcn_exp2f(x); }
__device__ __forceinline__ void wg_barrier() {
  asm volatile("s_waitcnt lgkmcnt(0)" ::: "memory");
  __builtin_amdgcn_s_barrier();
  asm volatile("" ::: "memory");
}
#define PACE() do { asm volatile("s_waitcnt vmcnt(%0)" :: "n"(KN_L - 1) : "memory"); __builtin_amdgcn_sched_barrier(0); } while (0)
#define FENCE() __builtin_amdgcn_sched_barrier(0)

__device__ __forceinline__ v4u rec_make(u32 a, u32 b, u32 t1, u32 t2) { return v4u{a, t1 ^ b, b, t2 ^ a}; }
__device__ __forceinline__ bool rec_ok(v4u r, u32 t1, u32 t2) { return ((r.y ^ r.z) == t1) & ((r.w ^ r.x) == t2); }
__device__ __forceinline__ v4u ld_sc1(__amdgpu_buffer_rsrc_t rs, int off) { return __builtin_amdgcn_raw_buffer_load_b128(rs, off, 0, 16); }
__device__ __forceinline__ void st_sc1(__amdgpu_buffer_rsrc_t rs, int off, v4u v) { __builtin_amdgcn_raw_buffer_store_b128(v, rs, off, 0, 16); }
constexpr u64 SPIN_CAP = 5000000ull;

__global__ void __launch_bounds__(NTHR) gqa_decode_kernel(const u16* __restrict__ x, const u16* __restrict__ kc, const u16* __restrict__ vc,
                                                          const u16* __restrict__ wq, const u16* __restrict__ wo, u16* __restrict__ out,
                                                          unsigned char* ws) {
  __shared__ __attribute__((aligned(16))) float lds[L_END];
  const int tid = threadIdx.x, lane = tid & 63, w = __builtin_amdgcn_readfirstlane(tid >> 6);
  const int bid = blockIdx.x;
  const int g = KN_MAP ? (bid & 7) : (bid >> 5);
  const int j = KN_MAP ? (bid >> 3) : (bid & 31);
  const int js = KN_MAP ? j : ((j & 7) * 4 + (j >> 3));
  const __amdgpu_buffer_rsrc_t rs = __builtin_amdgcn_make_buffer_rsrc((void*)ws, 0, WS_END, 0x00020000);

  if (w < NW) {
    const int g4 = lane >> 4;
    v4u xa[2]; u32 wb[2][8];
#pragma unroll
    for (int s = 0; s < 2; ++s) {
      const int r0 = 64 * w + 32 * s + 8 * g4;
      xa[s] = *(const GAS v4u*)(x + r0);
#pragma unroll
      for (int jj = 0; jj < 8; ++jj) wb[s][jj] = __builtin_nontemporal_load((const GAS u16*)wq + (size_t)(r0 + jj) * (HQ * DH) + g * 512 + js * 16 + (lane & 15));
    }
    FENCE();
    constexpr size_t BSTEP = (size_t)(KN_ORD ? 32 * NW : NW) * 8192;
    const size_t b0 = (size_t)g * (SK * DH * 2) + (size_t)(KN_ORD ? (j * NW + w) : (j * NW * NBLK + w)) * 8192;
    const char* kb = (const char*)kc + b0 + ((lane >> 2) & 3) * 256 + g4 * 64 + (lane & 3) * 16;
    const char* vb = (const char*)vc + b0 + lane * 16;
    v4u K[8], V[8];
#pragma unroll
    for (int t = 0; t < KN_L; ++t) K[t] = ld_nt(kb + t * 1024);
    FENCE();
#pragma unroll
    for (int s = 0; s < 2; ++s) asm volatile("" : "+v"(wb[s][0]), "+v"(wb[s][1]), "+v"(wb[s][2]), "+v"(wb[s][3]), "+v"(wb[s][4]), "+v"(wb[s][5]), "+v"(wb[s][6]), "+v"(wb[s][7]));
    {
      f32x4 qa = {0.f, 0.f, 0.f, 0.f};
#pragma unroll
      for (int s = 0; s < 2; ++s) {
        const v4u b = {(u32)wb[s][0] | ((u32)wb[s][1] << 16), (u32)wb[s][2] | ((u32)wb[s][3] << 16), (u32)wb[s][4] | ((u32)wb[s][5] << 16), (u32)wb[s][6] | ((u32)wb[s][7] << 16)};
        qa = mfma16(xa[s], b, qa);
      }
      if (lane < 16) lds[L_QRED + w * 16 + lane] = qa[0];
    }
    wg_barrier();
#pragma unroll
    for (int t = KN_L; t < 8; ++t) { PACE(); K[t] = ld_nt(kb + t * 1024); FENCE(); }
#pragma unroll
    for (int t = 0; t < 8; ++t) { PACE(); V[t] = ld_nt(vb + t * 1024); FENCE(); }
    wg_barrier();
    const v4u qf = *(const v4u*)&lds[L_QF + lane * 4];

    float m = -INFINITY, l = 0.f;
    f32x4 acc[8];
#pragma unroll
    for (int i = 0; i < 8; ++i) acc[i] = f32x4{0.f, 0.f, 0.f, 0.f};
    v4u P;

#define QK_SOFTMAX() do {                                                                                   \
      f32x4 d[8];                                                                                           \
      _Pragma("unroll") for (int t = 0; t < 8; ++t) d[t] = mfma16(K[t], qf, f32x4{0.f, 0.f, 0.f, 0.f});     \
      float sc[2];                                                                                          \
      _Pragma("unroll") for (int G = 0; G < 2; ++G) {                                                       \
        float T[4];                                                                                         \
        _Pragma("unroll") for (int r = 0; r < 4; ++r) {                                                     \
          float tt = d[4 * G + r][0];                                                                       \
          tt = bank_put<1>(tt, d[4 * G + ((1 + r) & 3)][1]);                                                \
          tt = bank_put<2>(tt, d[4 * G + ((2 + r) & 3)][2]);                                                \
          tt = bank_put<3>(tt, d[4 * G + ((3 + r) & 3)][3]);                                                \
          T[r] = tt; }                                                                                      \
        sc[G] = (T[0] + dppf<ROR4>(T[1])) + (dppf<ROR8>(T[2]) + dppf<ROR12>(T[3])); }                       \
      float mx = fmaxf(sc[0], sc[1]);                                                                       \
      mx = fmaxf(mx, dppf<ROR4>(mx)); mx = fmaxf(mx, dppf<ROR8>(mx)); mx = xrow_max(mx);                    \
      const float mn = fmaxf(m, mx * C2);                                                                   \
      const float al = e2(m - mn);                                                                          \
      m = mn;                                                                                               \
      const float p0 = e2(fmaf(sc[0], C2, -mn)), p1 = e2(fmaf(sc[1], C2, -mn));                             \
      l = fmaf(l, al, p0 + p1);                                                                             \
      _Pragma("unroll") for (int i = 0; i < 8; ++i) acc[i] *= al;                                           \
      const u32 pk = pack_bf16(p0, p1);                                                                     \
      P = v4u{pk, dppu<ROR12>(pk), dppu<ROR8>(pk), dppu<ROR4>(pk)};                                         \
    } while (0)

#define PV() do {                                                                                           \
      _Pragma("unroll") for (int e = 0; e < 4; ++e) {                                                       \
        const v4u lo = {__builtin_amdgcn_perm(V[4][e], V[0][e], 0x05040100u), __builtin_amdgcn_perm(V[5][e], V[1][e], 0x05040100u),  \
                        __builtin_amdgcn_perm(V[6][e], V[2][e], 0x05040100u), __builtin_amdgcn_perm(V[7][e], V[3][e], 0x05040100u)}; \
        const v4u hi = {__builtin_amdgcn_perm(V[4][e], V[0][e], 0x07060302u), __builtin_amdgcn_perm(V[5][e], V[1][e], 0x07060302u),  \
                        __builtin_amdgcn_perm(V[6][e], V[2][e], 0x07060302u), __builtin_amdgcn_perm(V[7][e], V[3][e], 0x07060302u)}; \
        acc[2 * e] = mfma16(lo, P, acc[2 * e]);                                                             \
        acc[2 * e + 1] = mfma16(hi, P, acc[2 * e + 1]); }                                                   \
    } while (0)

    QK_SOFTMAX();
    FENCE();
#pragma unroll 1
    for (int n = 1; n < NBLK; ++n) {
      kb += BSTEP; vb += BSTEP;
#pragma unroll
      for (int t = 0; t < 8; ++t) { PACE(); K[t] = ld_nt(kb + t * 1024); FENCE(); if (t == KN_L) { PV(); FENCE(); } }
#pragma unroll
      for (int t = 0; t < 8; ++t) { PACE(); V[t] = ld_nt(vb + t * 1024); FENCE(); if (t == KN_L) { QK_SOFTMAX(); FENCE(); } }
    }
    PV();

    l += dppf<ROR4>(l); l += dppf<ROR8>(l); l = xrow_sum(l);
    if ((lane & 12) == 0) {
      const int h = lane & 3;
      float* row = &lds[L_PART + (w * 4 + h) * PROW + 32 * g4];
#pragma unroll
      for (int r = 0; r < 4; ++r) {
        *(f32x4*)(row + 8 * r) = f32x4{acc[0][r], acc[1][r], acc[2][r], acc[3][r]};
        *(f32x4*)(row + 8 * r + 4) = f32x4{acc[4][r], acc[5][r], acc[6][r], acc[7][r]};
      }
      if (g4 == 0) *(f32x2*)&lds[L_ML + (w * 4 + h) * 2] = f32x2{m, l};
    }
    wg_barrier();
    if (w < 4) {
      const u32 t1 = __float_as_uint(lds[L_TAG]), t2 = __float_as_uint(lds[L_TAG + 1]);
      f32x2 ml[NW], pv[NW];
#pragma unroll
      for (int i = 0; i < NW; ++i) { ml[i] = *(const f32x2*)&lds[L_ML + (i * 4 + w) * 2]; pv[i] = *(const f32x2*)&lds[L_PART + (i * 4 + w) * PROW + 2 * lane]; }
      float M = ml[0].x;
#pragma unroll
      for (int i = 1; i < NW; ++i) M = fmaxf(M, ml[i].x);
      float L = 0.f, o0 = 0.f, o1 = 0.f;
#pragma unroll
      for (int i = 0; i < NW; ++i) { const float e = e2(ml[i].x - M); L = fmaf(e, ml[i].y, L); o0 = fmaf(e, pv[i].x, o0); o1 = fmaf(e, pv[i].y, o1); }
      st_sc1(rs, WS_P1 + ((g * 32 + w * 8 + (lane >> 3)) * 32 + j) * 128 + (lane & 7) * 16, rec_make(__float_as_uint(o0), __float_as_uint(o1), t1, t2));
      if (lane == 0) st_sc1(rs, WS_ML + ((g * 4 + w) * 32 + j) * 16, rec_make(__float_as_uint(M), __float_as_uint(L), t1, t2));
    }
  } else {
    const u64 ep = __hip_atomic_load((GAS u64*)(ws + WS_EPOCH), __ATOMIC_RELAXED, __HIP_MEMORY_SCOPE_AGENT);
    u64 z = ep + 0x9E3779B97F4A7C15ull;
    z = (z ^ (z >> 30)) * 0xBF58476D1CE4E5B9ull; z = (z ^ (z >> 27)) * 0x94D049BB133111EBull; z ^= z >> 31;
    const u32 t1 = __builtin_amdgcn_readfirstlane((u32)z) | 1u, t2 = __builtin_amdgcn_readfirstlane((u32)(z >> 32));
    if (lane == 0) { lds[L_TAG] = __uint_as_float(t1); lds[L_TAG + 1] = __uint_as_float(t2); }
    wg_barrier();
    if (lane < 4) {
      f32x4 s = *(const f32x4*)&lds[L_QRED + lane * 4];
#pragma unroll
      for (int i = 1; i < NW; ++i) s += *(const f32x4*)&lds[L_QRED + i * 16 + lane * 4];
      st_sc1(rs, WS_QX + (g * 128 + js * 4 + lane) * 16, rec_make(pack_bf16(s[0], s[1]), pack_bf16(s[2], s[3]), t1, t2));
    }
    {
      const int off = WS_QX + (g * 128 + (lane & 3) * 32 + (lane >> 4) * 8 + ((lane >> 2) & 3) * 2) * 16;
      const u64 tb = __builtin_amdgcn_s_memrealtime();
      v4u r0, r1;
      for (;;) {
        asm volatile("" ::: "memory");
        r0 = ld_sc1(rs, off); r1 = ld_sc1(rs, off + 16);
        if (__all(rec_ok(r0, t1, t2) & rec_ok(r1, t1, t2))) break;
        if (__builtin_amdgcn_s_memrealtime() - tb > SPIN_CAP) break;
        __builtin_amdgcn_s_sleep(2);
      }
      *(v4u*)&lds[L_QF + lane * 4] = v4u{r0.x, r0.z, r1.x, r1.z};
    }
    wg_barrier();
    u32 wp[8][8];
    {
      v4u wr[16];
#pragma unroll
      for (int r = 0; r < 16; ++r) wr[r] = ld_nt(wo + (size_t)(g * 512 + js * 16 + r) * DM + lane * 8);
#pragma unroll
      for (int r = 0; r < 8; ++r)
#pragma unroll
        for (int e = 0; e < 4; ++e) {
          wp[r][2 * e] = __builtin_amdgcn_perm(wr[2 * r + 1][e], wr[2 * r][e], 0x05040100u);
          wp[r][2 * e + 1] = __builtin_amdgcn_perm(wr[2 * r + 1][e], wr[2 * r][e], 0x07060302u);
        }
#pragma unroll
      for (int r = 0; r < 8; ++r) asm volatile("" : "+v"(wp[r][0]), "+v"(wp[r][1]), "+v"(wp[r][2]), "+v"(wp[r][3]), "+v"(wp[r][4]), "+v"(wp[r][5]), "+v"(wp[r][6]), "+v"(wp[r][7]));
    }
    wg_barrier();
    u32 opk;
    {
      const int offv = WS_P1 + ((g * 32 + js) * 32 + (lane >> 3) * 4) * 128 + (lane & 7) * 16;
      const int offm = WS_ML + ((g * 4 + (js >> 3)) * 32 + (lane >> 3) * 4) * 16;
      const u64 tb = __builtin_amdgcn_s_memrealtime();
      v4u rv[4], rm[4];
      for (;;) {
        asm volatile("" ::: "memory");
#pragma unroll
        for (int i = 0; i < 4; ++i) { rv[i] = ld_sc1(rs, offv + i * 128); rm[i] = ld_sc1(rs, offm + i * 16); }
        bool ok = true;
#pragma unroll
        for (int i = 0; i < 4; ++i) ok &= rec_ok(rv[i], t1, t2) & rec_ok(rm[i], t1, t2);
        if (__all(ok)) break;
        if (__builtin_amdgcn_s_memrealtime() - tb > SPIN_CAP) break;
        __builtin_amdgcn_s_sleep(2);
      }
      float M = fmaxf(fmaxf(__uint_as_float(rm[0].x), __uint_as_float(rm[1].x)), fmaxf(__uint_as_float(rm[2].x), __uint_as_float(rm[3].x)));
      M = fmaxf(M, dppf<ROR8>(M)); M = xrow_max(M);
      float L = 0.f, o0 = 0.f, o1 = 0.f;
#pragma unroll
      for (int i = 0; i < 4; ++i) {
        const float e = e2(__uint_as_float(rm[i].x) - M);
        L = fmaf(e, __uint_as_float(rm[i].z), L); o0 = fmaf(e, __uint_as_float(rv[i].x), o0); o1 = fmaf(e, __uint_as_float(rv[i].z), o1);
      }
      L += dppf<ROR8>(L); o0 += dppf<ROR8>(o0); o1 += dppf<ROR8>(o1);
      L = xrow_sum(L); o0 = xrow_sum(o0); o1 = xrow_sum(o1);
      const float inv = 1.0f / L;
      opk = pack_bf16(o0 * inv, o1 * inv);
    }
    float y[8];
#pragma unroll
    for (int c = 0; c < 8; ++c) y[c] = 0.f;
#pragma unroll
    for (int r = 0; r < 8; ++r) {
      const u32 s = __builtin_amdgcn_readlane(opk, r);
#pragma unroll
      for (int c = 0; c < 8; ++c) y[c] = __builtin_amdgcn_fdot2_f32_bf16(__builtin_bit_cast(bf16x2, wp[r][c]), __builtin_bit_cast(bf16x2, s), y[c], false);
    }
#pragma unroll
    for (int i = 0; i < 4; ++i) st_sc1(rs, WS_P2 + ((lane * 4 + i) * 256 + bid) * 16, rec_make(__float_as_uint(y[2 * i]), __float_as_uint(y[2 * i + 1]), t1, t2));
    {
      const int off = WS_P2 + (bid * 256 + lane) * 16;
      const u64 tb = __builtin_amdgcn_s_memrealtime();
      v4u r[4];
      for (;;) {
        asm volatile("" ::: "memory");
#pragma unroll
        for (int i = 0; i < 4; ++i) r[i] = ld_sc1(rs, off + i * 1024);
        if (__all(rec_ok(r[0], t1, t2) & rec_ok(r[1], t1, t2) & rec_ok(r[2], t1, t2) & rec_ok(r[3], t1, t2))) break;
        if (__builtin_amdgcn_s_memrealtime() - tb > SPIN_CAP) break;
        __builtin_amdgcn_s_sleep(2);
      }
      float a = (__uint_as_float(r[0].x) + __uint_as_float(r[1].x)) + (__uint_as_float(r[2].x) + __uint_as_float(r[3].x));
      float b = (__uint_as_float(r[0].z) + __uint_as_float(r[1].z)) + (__uint_as_float(r[2].z) + __uint_as_float(r[3].z));
      a += dppf<XOR1>(a); b += dppf<XOR1>(b); a += dppf<XOR2>(a); b += dppf<XOR2>(b);
      a += dppf<ROR4>(a); b += dppf<ROR4>(b); a += dppf<ROR8>(a); b += dppf<ROR8>(b);
      a = xrow_sum(a); b = xrow_sum(b);
      if (lane == 0) {
        *(GAS u32*)(out + 2 * bid) = pack_bf16(a, b);
        if (bid == 0) __hip_atomic_store((GAS u64*)(ws + WS_EPOCH), ep + 1ull, __ATOMIC_RELAXED, __HIP_MEMORY_SCOPE_AGENT);
      }
    }
  }
}

extern "C" void kernel_launch(void* const* d_in, const int* in_sizes, int n_in, void* d_out, int out_size, void* d_ws, size_t ws_size,
                              hipStream_t stream) {
  gqa_decode_kernel<<<NWG, NTHR, 0, stream>>>((const u16*)d_in[0], (const u16*)d_in[1], (const u16*)d_in[2], (const u16*)d_in[3],
                                              (const u16*)d_in[4], (u16*)d_out, (unsigned char*)d_ws);
}
```

```cpp
#include <hip/hip_runtime.h>
#include <hip/hip_bf16.h>
#include <stdint.h>

typedef unsigned short u16;
typedef unsigned int u32;
typedef unsigned long long u64;
typedef u32 v4u __attribute__((ext_vector_type(4)));
typedef float f32x4 __attribute__((ext_vector_type(4)));
typedef float f32x2 __attribute__((ext_vector_type(2)));
typedef __bf16 bf16x8 __attribute__((ext_vector_type(8)));
typedef __bf16 bf16x2 __attribute__((ext_vector_type(2)));
#define GAS __attribute__((address_space(1)))
#define LAS __attribute__((address_space(3)))

constexpr int DM = 512, HQ = 32, SK = 65536, DH = 128;
constexpr int NWG = 256, NW = 8, NTHR = (NW + 1) * 64;
constexpr int NBLK = SK / 32 / 32 / NW;
constexpr float C2 = 0.088388347648318440f * 1.4426950408889634f;

#define KV_PAT "axwwwyypxqrxcb"
constexpr int pat_stride(char ch, int idx) {
  for (int i = 0; i < 14; ++i) if (KV_PAT[i] == ch && idx-- == 0) return 1024 << i;
  return 0;
}
constexpr int toff(int t) { return (t & 1) * pat_stride('a', 0) + ((t >> 1) & 1) * pat_stride('b', 0) + (t >> 2) * pat_stride('c', 0); }
__device__ __forceinline__ int noff(int n) { return (n & 1) * pat_stride('p', 0) + ((n >> 1) & 1) * pat_stride('q', 0) + (n >> 2) * pat_stride('r', 0); }

constexpr int G_S0 = 145, G_PER = 215;
constexpr int G_D2 = 90;
constexpr int G_C = 75;
constexpr int G_TL = 120;
constexpr int T_Q = 600, T_ML = 300;
constexpr int g_due(int hb) { return G_S0 + ((hb * G_PER) >> 1); }
constexpr u64 SPIN_CAP = 20000000ull;

constexpr int WS_EPOCH = 0;
constexpr int WS_ML = 4096;
constexpr int WS_QX = 32768;
constexpr int WS_P1 = 262144;
constexpr int WS_P2 = WS_P1 + (1 << 20);
constexpr int WS_END = WS_P2 + (1 << 20);

constexpr int PROW = 132, POFF = 544;
constexpr int WREG = POFF + 512;
constexpr int L_W = 0;
constexpr int L_ML = L_W + NW * WREG;
constexpr int L_F = L_ML + NW * 8;
constexpr int L_QF = L_F + NW * 4;
constexpr int L_CTL = L_QF + 256;
constexpr int L_FIN = L_CTL + 8;
constexpr int L_END = L_FIN + NW * 16;
constexpr int CW_NML = 0, CW_NLAST = 1, CW_QRDY = 2, CW_TG = 3, CW_T1 = 4, CW_T2 = 5;

__device__ __forceinline__ f32x4 mfma16(v4u a, v4u b, f32x4 c) {
  return __builtin_amdgcn_mfma_f32_16x16x32_bf16(__builtin_bit_cast(bf16x8, a), __builtin_bit_cast(bf16x8, b), c, 0, 0, 0);
}
__device__ __forceinline__ u32 pack_bf16(float lo, float hi) {
  bf16x2 r; r.x = (__bf16)lo; r.y = (__bf16)hi; return __builtin_bit_cast(u32, r);
}
__device__ __forceinline__ float dot2(u32 a, u32 b, float c) {
  return __builtin_amdgcn_fdot2_f32_bf16(__builtin_bit_cast(bf16x2, a), __builtin_bit_cast(bf16x2, b), c, false);
}
__device__ __forceinline__ u32 lo16s(u32 a, u32 b) { return __builtin_amdgcn_perm(b, a, 0x05040100u); }
__device__ __forceinline__ u32 hi16s(u32 a, u32 b) { return __builtin_amdgcn_perm(b, a, 0x07060302u); }
template <int CTRL> __device__ __forceinline__ float dppf(float x) {
  return __builtin_bit_cast(float, __builtin_amdgcn_mov_dpp(__builtin_bit_cast(int, x), CTRL, 0xf, 0xf, true));
}
template <int BANK> __device__ __forceinline__ float bank_put(float old, float src) {
  return __builtin_bit_cast(float, __builtin_amdgcn_update_dpp(__builtin_bit_cast(int, old), __builtin_bit_cast(int, src), 0xE4, 0xf, 1 << BANK, false));
}
constexpr int ROR4 = 0x124, ROR8 = 0x128, ROR12 = 0x12C, XOR1 = 0xB1, XOR2 = 0x4E;
__device__ __forceinline__ float xrow_max(float x) {
  auto s = __builtin_amdgcn_permlane16_swap(__float_as_uint(x), __float_as_uint(x), false, false);
  x = fmaxf(__uint_as_float(s[0]), __uint_as_float(s[1]));
  auto t = __builtin_amdgcn_permlane32_swap(__float_as_uint(x), __float_as_uint(x), false, false);
  return fmaxf(__uint_as_float(t[0]), __uint_as_float(t[1]));
}
__device__ __forceinline__ float xrow_sum(float x) {
  auto s = __builtin_amdgcn_permlane16_swap(__float_as_uint(x), __float_as_uint(x), false, false);
  x = __uint_as_float(s[0]) + __uint_as_float(s[1]);
  auto t = __builtin_amdgcn_permlane32_swap(__float_as_uint(x), __float_as_uint(x), false, false);
  return __uint_as_float(t[0]) + __uint_as_float(t[1]);
}
__device__ __forceinline__ float e2(float x) { return __builtin_amdgcn_exp2f(x); }
__device__ __forceinline__ u64 now() { return __builtin_amdgcn_s_memrealtime(); }
__device__ __forceinline__ void wg_barrier() {
  asm volatile("s_waitcnt lgkmcnt(0)" ::: "memory");
  __builtin_amdgcn_s_barrier();
  asm volatile("" ::: "memory");
}
#define FENCE() __builtin_amdgcn_sched_barrier(0)
__device__ __forceinline__ u32 cw_load(float* lds, int i) { return __hip_atomic_load((u32*)&lds[L_CTL + i], __ATOMIC_RELAXED, __HIP_MEMORY_SCOPE_WORKGROUP); }
__device__ __forceinline__ void cw_store(float* lds, int i, u32 v) { __hip_atomic_store((u32*)&lds[L_CTL + i], v, __ATOMIC_RELAXED, __HIP_MEMORY_SCOPE_WORKGROUP); }
__device__ __forceinline__ void cw_inc(float* lds, int i) { __hip_atomic_fetch_add((u32*)&lds[L_CTL + i], 1u, __ATOMIC_RELAXED, __HIP_MEMORY_SCOPE_WORKGROUP); }
template <int SLEEP> __device__ __forceinline__ void cw_wait(float* lds, int i, u32 want) {
  const u64 tb = now();
  while (cw_load(lds, i) < want) {
    if (now() - tb > 2 * SPIN_CAP) break;
    __builtin_amdgcn_s_sleep(SLEEP);
  }
  asm volatile("" ::: "memory");
}

__device__ __forceinline__ v4u rec_make(u32 a, u32 b, u32 t1, u32 t2) { return v4u{a, t1 ^ b, b, t2 ^ a}; }
__device__ __forceinline__ u32 rec_bad(v4u r, u32 t1, u32 t2) { return (r.y ^ r.z ^ t1) | (r.w ^ r.x ^ t2); }
constexpr int AUX_NT = 2, AUX_SC1 = 16;
__device__ __forceinline__ v4u ld_sc1(__amdgpu_buffer_rsrc_t rs, int off) { return __builtin_amdgcn_raw_buffer_load_b128(rs, off, 0, AUX_SC1); }
__device__ __forceinline__ void st_sc1(__amdgpu_buffer_rsrc_t rs, int off, v4u v) { __builtin_amdgcn_raw_buffer_store_b128(v, rs, off, 0, AUX_SC1); }
__device__ __forceinline__ f32x2 gather_sum(__amdgpu_buffer_rsrc_t rs, int off, u32 t1, u32 t2) {
  const u64 tb = now();
  v4u r[4];
  bool ok = false;
  for (;;) {
    asm volatile("" ::: "memory");
    if (!ok) {
#pragma unroll
      for (int i = 0; i < 4; ++i) r[i] = ld_sc1(rs, off + i * 128);
      ok = (rec_bad(r[0], t1, t2) | rec_bad(r[1], t1, t2) | rec_bad(r[2], t1, t2) | rec_bad(r[3], t1, t2)) == 0u;
    }
    if (__all(ok)) break;
    if (now() - tb > SPIN_CAP) break;
  }
  float a = (__uint_as_float(r[0].x) + __uint_as_float(r[1].x)) + (__uint_as_float(r[2].x) + __uint_as_float(r[3].x));
  float b = (__uint_as_float(r[0].z) + __uint_as_float(r[1].z)) + (__uint_as_float(r[2].z) + __uint_as_float(r[3].z));
  a += dppf<ROR8>(a); b += dppf<ROR8>(b);
  return f32x2{xrow_sum(a), xrow_sum(b)};
}

__global__ void __launch_bounds__(NTHR, 3) gqa_decode_kernel(const u16* __restrict__ x, const u16* __restrict__ kc, const u16* __restrict__ vc,
                                                             const u16* __restrict__ wq, const u16* __restrict__ wo, u16* __restrict__ out,
                                                             unsigned char* ws) {
  __shared__ __attribute__((aligned(16))) float lds[L_END];
  asm volatile("" :: "s"(x), "s"(kc), "s"(vc), "s"(wq), "s"(wo), "s"(out), "s"(ws));
  const int tid = threadIdx.x, lane = tid & 63, wv = __builtin_amdgcn_readfirstlane(tid >> 6);
  const int bid = blockIdx.x, g = bid >> 5, j = bid & 31;
  const bool is_fin = (bid & 7) == ((bid >> 3) & 7);
  const int fin = bid >> 3;
  const __amdgpu_buffer_rsrc_t rs = __builtin_amdgcn_make_buffer_rsrc((void*)ws, 0, WS_END, 0x00020000);

  if (wv > 0) {
    const __amdgpu_buffer_rsrc_t rk = __builtin_amdgcn_make_buffer_rsrc((void*)(kc + (size_t)g * (SK * DH)), 0, SK * DH * 2, 0x00020000);
    const __amdgpu_buffer_rsrc_t rv = __builtin_amdgcn_make_buffer_rsrc((void*)(vc + (size_t)g * (SK * DH)), 0, SK * DH * 2, 0x00020000);
    const int w = wv - 1, g4 = lane >> 4;
    const int kl = ((lane >> 2) & 3) * 256 + g4 * 64 + (lane & 3) * 16, vl = lane * 16;
    const int sb = (j & 1) * pat_stride('x', 0) + ((j >> 1) & 1) * pat_stride('x', 1) + ((j >> 2) & 1) * pat_stride('x', 2) + ((j >> 3) & 1) * pat_stride('y', 0) +
                   (j >> 4) * pat_stride('y', 1) + (w & 1) * pat_stride('w', 0) + ((w >> 1) & 1) * pat_stride('w', 1) + (w >> 2) * pat_stride('w', 2);
#define LDK(n, t) __builtin_amdgcn_raw_buffer_load_b128(rk, kl, sb + noff(n) + toff(t), AUX_NT)
#define LDV(n, t) __builtin_amdgcn_raw_buffer_load_b128(rv, vl, sb + noff(n) + toff(t), AUX_NT)
    u32 tg = (u32)now();
    int lead = 0;
    const int gd2 = G_D2;
#define PACE(blk, t) do {                                                                                                                  \
      if (((t) & 3) == 0) { lead = g_due(2 * (blk) + ((t) >> 2)) - (int)((u32)now() - tg); FENCE(); }                                      \
      if ((blk) >= 3)                                                                                                                      \
        asm volatile("s_waitcnt vmcnt(3)\n\ts_cmp_gt_i32 %0, 0\n\ts_cbranch_scc0 1f\n\ts_waitcnt vmcnt(2)\n\ts_cmp_gt_i32 %0, %1\n\t"      \
                     "s_cbranch_scc0 1f\n\ts_waitcnt vmcnt(1)\n1:" :: "s"(lead), "s"(gd2) : "memory", "scc");                              \
      else                                                                                                                                 \
        asm volatile("s_waitcnt vmcnt(3)\n\ts_cmp_gt_i32 %0, 0\n\ts_cbranch_scc0 1f\n\ts_waitcnt vmcnt(2)\n1:" :: "s"(lead) : "memory", "scc"); \
      FENCE(); } while (0)
    v4u A[8], B[8], C[8], D[8];
    float* const wreg = &lds[L_W + w * WREG];
#pragma unroll
    for (int t = 0; t < 8; ++t) {
      if (t >= 4) { asm volatile("s_waitcnt vmcnt(3)" ::: "memory"); FENCE(); }
      A[t] = LDK(0, t); FENCE();
      if (t == 3) { wg_barrier(); FENCE(); }
    }
#pragma unroll
    for (int t = 0; t < 8; ++t) { PACE(1, t); B[t] = LDK(1, t); FENCE(); }
#pragma unroll
    for (int t = 0; t < 8; ++t) { PACE(2, t); C[t] = LDK(2, t); FENCE(); }
#pragma unroll
    for (int t = 0; t < 8; ++t) { PACE(3, t); D[t] = LDK(3, t); FENCE(); }
    cw_wait<1>(lds, CW_QRDY, 1u);
    const v4u qf = *(const v4u*)&lds[L_QF + (lane & 3) * 64 + g4 * 16 + ((lane >> 2) & 3) * 4];
    float sc[NBLK][2];

#define QKH(KK, n, G) do {                                                                                  \
      f32x4 d[4];                                                                                           \
      _Pragma("unroll") for (int t = 0; t < 4; ++t) d[t] = mfma16(KK[4 * (G) + t], qf, f32x4{0.f, 0.f, 0.f, 0.f}); \
      float T[4];                                                                                           \
      _Pragma("unroll") for (int r = 0; r < 4; ++r) {                                                       \
        float tt = d[r][0];                                                                                 \
        tt = bank_put<1>(tt, d[(1 + r) & 3][1]);                                                            \
        tt = bank_put<2>(tt, d[(2 + r) & 3][2]);                                                            \
        tt = bank_put<3>(tt, d[(3 + r) & 3][3]);                                                            \
        T[r] = tt; }                                                                                        \
      sc[n][G] = (T[0] + dppf<ROR4>(T[1])) + (dppf<ROR8>(T[2]) + dppf<ROR12>(T[3]));                        \
      asm volatile("" : "+v"(sc[n][G]));                                                                    \
      FENCE();                                                                                              \
    } while (0)
#define PV(VV, n) do {                                                                                      \
      const v4u P = *(const v4u*)(wreg + POFF + (n) * 64 + g4 * 16 + (lane & 3) * 4);                       \
      _Pragma("unroll") for (int e = 0; e < 4; ++e) {                                                       \
        const v4u lo = {lo16s(VV[0][e], VV[4][e]), lo16s(VV[1][e], VV[5][e]), lo16s(VV[2][e], VV[6][e]), lo16s(VV[3][e], VV[7][e])}; \
        const v4u hi = {hi16s(VV[0][e], VV[4][e]), hi16s(VV[1][e], VV[5][e]), hi16s(VV[2][e], VV[6][e]), hi16s(VV[3][e], VV[7][e])}; \
        acc[2 * e] = mfma16(lo, P, acc[2 * e]);                                                             \
        acc[2 * e + 1] = mfma16(hi, P, acc[2 * e + 1]); }                                                   \
      FENCE();                                                                                              \
    } while (0)
#define STEP(X, nx, Y, ny) _Pragma("unroll") for (int t = 0; t < 8; ++t) { PACE(nx, t); X[t] = LDK(nx, t); FENCE(); if (t == 2) QKH(Y, ny, 0); if (t == 6) QKH(Y, ny, 1); }

    QKH(A, 0, 0);
#pragma unroll
    for (int t = 0; t < 8; ++t) {
      PACE(4, t); A[t] = LDK(4, t); FENCE();
      if (t == 0) QKH(A, 0, 1);
      if (t == 1) QKH(B, 1, 0);
      if (t == 2) QKH(B, 1, 1);
      if (t == 3) QKH(C, 2, 0);
      if (t == 4) QKH(C, 2, 1);
      if (t == 5) QKH(D, 3, 0);
      if (t == 6) QKH(D, 3, 1);
    }
    STEP(B, 5, A, 4)
    STEP(A, 6, B, 5)
    STEP(B, 7, A, 6)
#pragma unroll
    for (int t = 0; t < 8; ++t) {
      PACE(8, t); A[t] = LDV(0, t); FENCE();
      if (t == 2) QKH(B, 7, 0);
      if (t == 4) QKH(B, 7, 1);
      if (t == 5) {
        float mx = fmaxf(sc[0][0], sc[0][1]);
#pragma unroll
        for (int n = 1; n < NBLK; ++n) mx = fmaxf(mx, fmaxf(sc[n][0], sc[n][1]));
        mx = fmaxf(mx, dppf<ROR4>(mx)); mx = fmaxf(mx, dppf<ROR8>(mx)); mx = xrow_max(mx);
        const float m = mx * C2;
        float l = 0.f;
#pragma unroll
        for (int n = 0; n < NBLK; ++n) {
          const float p0 = e2(fmaf(sc[n][0], C2, -m)), p1 = e2(fmaf(sc[n][1], C2, -m));
          l += p0 + p1;
          *(u32*)(wreg + POFF + n * 64 + g4 * 16 + (lane & 3) * 4 + ((lane >> 2) & 3)) = pack_bf16(p0, p1);
        }
        l += dppf<ROR4>(l); l += dppf<ROR8>(l); l = xrow_sum(l);
        if (lane < 4) *(f32x2*)&lds[L_ML + (lane * NW + w) * 2] = f32x2{m, l};
        asm volatile("s_waitcnt lgkmcnt(0)" ::: "memory");
        if (lane == 0) cw_inc(lds, CW_NML);
        FENCE();
      }
    }
    f32x4 acc[8];
#pragma unroll
    for (int i = 0; i < 8; ++i) acc[i] = f32x4{0.f, 0.f, 0.f, 0.f};
#pragma unroll 1
    for (int n = 1; n < NBLK - 1; n += 2) {
#pragma unroll
      for (int t = 0; t < 8; ++t) {
        PACE(8 + n, t); B[t] = LDV(n, t); FENCE();
        if (t == 6) { tg = __builtin_amdgcn_readfirstlane(cw_load(lds, CW_TG)); PV(A, n - 1); }
      }
#pragma unroll
      for (int t = 0; t < 8; ++t) {
        PACE(9 + n, t); A[t] = LDV(n + 1, t); FENCE();
        if (t == 6) { tg = __builtin_amdgcn_readfirstlane(cw_load(lds, CW_TG)); PV(B, n); }
      }
    }
#pragma unroll
    for (int t = 0; t < 8; ++t) {
      PACE(15, t); B[t] = LDV(NBLK - 1, t); FENCE();
      if (t == 6) PV(A, NBLK - 2);
    }
    if (lane == 0) cw_inc(lds, CW_NLAST);
    FENCE();
    const u32 t1 = cw_load(lds, CW_T1), t2 = cw_load(lds, CW_T2);
    int p1off = WS_P1 + ((g * 32 + (w & 3) * 8 + g4 * 2 + (lane & 1)) * 32 + j) * 128 + ((lane >> 1) & 7) * 16;
    LAS f32x4* rowp = (LAS f32x4*)(wreg + (lane & 3) * PROW + 32 * g4);
    const LAS f32x2* rdp = (const LAS f32x2*)&lds[L_W + (w & 3) * PROW + 2 * lane];
    const LAS f32x4* fp = (const LAS f32x4*)&lds[L_F + (w & 3) * NW];
    {
      v4u P = *(const v4u*)(wreg + POFF + (NBLK - 1) * 64 + g4 * 16 + (lane & 3) * 4);
      asm volatile("" : "+v"(P), "+v"(p1off), "+v"(rowp), "+v"(rdp), "+v"(fp));
      v4u fr[8];
#pragma unroll
      for (int u = 0; u < 3; ++u) {
#pragma unroll
        for (int e = 0; e < 4; ++e) { fr[2 * e][u] = lo16s(B[u][e], B[u + 4][e]); fr[2 * e + 1][u] = hi16s(B[u][e], B[u + 4][e]); }
        asm volatile("" : "+v"(fr[0][u]), "+v"(fr[1][u]), "+v"(fr[2][u]), "+v"(fr[3][u]), "+v"(fr[4][u]), "+v"(fr[5][u]), "+v"(fr[6][u]), "+v"(fr[7][u]));
        FENCE();
      }
#pragma unroll
      for (int e = 0; e < 4; ++e) { fr[2 * e][3] = lo16s(B[3][e], B[7][e]); fr[2 * e + 1][3] = hi16s(B[3][e], B[7][e]); }
#pragma unroll
      for (int i = 0; i < 8; ++i) acc[i] = mfma16(fr[i], P, acc[i]);
      FENCE();
#pragma unroll
      for (int i = 0; i < 8; ++i) rowp[i] = acc[i];
    }
    wg_barrier();
    if (w < 4) {
      f32x2 pv[NW];
#pragma unroll
      for (int i = 0; i < NW; ++i) pv[i] = rdp[i * (WREG / 2)];
      const f32x4 fa = fp[0], fb = fp[1];
      const f32x2 o = ((pv[0] * fa[0] + pv[1] * fa[1]) + (pv[2] * fa[2] + pv[3] * fa[3])) + ((pv[4] * fb[0] + pv[5] * fb[1]) + (pv[6] * fb[2] + pv[7] * fb[3]));
      st_sc1(rs, p1off, rec_make(__float_as_uint(o.x), __float_as_uint(o.y), t1, t2));
    }
    if (is_fin) {
      int off = WS_P2 + (fin * 256 + w * 32 + (lane >> 3) * 4) * 128 + (lane & 7) * 16;
      asm volatile("" : "+v"(off));
      wg_barrier();
      __builtin_amdgcn_s_sleep(8);
      const f32x2 s = gather_sum(rs, off, t1, t2);
      if (lane < 8) *(f32x2*)&lds[L_FIN + w * 16 + lane * 2] = s;
      wg_barrier();
    }
  } else {
    __builtin_amdgcn_s_setprio(3);
    const u64 t0 = now();
    if (lane == 0) *(v4u*)&lds[L_CTL] = v4u{0u, 0u, 0u, (u32)t0};
    wg_barrier();
    FENCE();
    v4u wr[16], xr[2];
    {
      const __amdgpu_buffer_rsrc_t rq = __builtin_amdgcn_make_buffer_rsrc((void*)wq, 0, DM * HQ * DH * 2, 0x00020000);
      const int row0 = (j & 3) * 128 + (lane >> 3) * 16;
      const int vo = row0 * (HQ * DH * 2) + g * 1024 + (j >> 2) * 128 + (lane & 7) * 16;
#pragma unroll
      for (int r = 0; r < 16; ++r) wr[r] = __builtin_amdgcn_raw_buffer_load_b128(rq, vo, r * (HQ * DH * 2), AUX_NT);
      xr[0] = *(const GAS v4u*)(x + row0); xr[1] = *(const GAS v4u*)(x + row0 + 8);
    }
    const u64 ep = __hip_atomic_load((GAS u64*)(ws + WS_EPOCH), __ATOMIC_RELAXED, __HIP_MEMORY_SCOPE_AGENT);
    FENCE();
    float qv[8];
#pragma unroll
    for (int c = 0; c < 8; ++c) qv[c] = 0.f;
#pragma unroll
    for (int r = 0; r < 8; ++r)
#pragma unroll
      for (int e = 0; e < 4; ++e) {
        qv[2 * e] = dot2(lo16s(wr[2 * r][e], wr[2 * r + 1][e]), xr[r >> 2][r & 3], qv[2 * e]);
        qv[2 * e + 1] = dot2(hi16s(wr[2 * r][e], wr[2 * r + 1][e]), xr[r >> 2][r & 3], qv[2 * e + 1]);
      }
    float q4[4];
#pragma unroll
    for (int c = 0; c < 4; ++c) {
      const float a = qv[c] + dppf<ROR8>(qv[c]), b = qv[c + 4] + dppf<ROR8>(qv[c + 4]);
      q4[c] = xrow_sum((lane & 8) ? b : a);
    }
    u64 z = ep + 0x9E3779B97F4A7C15ull;
    z = (z ^ (z >> 30)) * 0xBF58476D1CE4E5B9ull; z = (z ^ (z >> 27)) * 0x94D049BB133111EBull; z ^= z >> 31;
    const u32 t1 = __builtin_amdgcn_readfirstlane((u32)z) | 1u, t2 = __builtin_amdgcn_readfirstlane((u32)(z >> 32));
    if (lane < 32)
      st_sc1(rs, WS_QX + ((g * 4 + (j & 3)) * 256 + (j >> 2) * 32 + (lane & 7) * 4 + ((lane >> 3) & 1) * 2 + (lane >> 4)) * 16,
             rec_make(__float_as_uint((lane & 16) ? q4[2] : q4[0]), __float_as_uint((lane & 16) ? q4[3] : q4[1]), t1, t2));
    if (lane == 0) { cw_store(lds, CW_T1, t1); cw_store(lds, CW_T2, t2); }
    {
      while (now() - t0 < T_Q) __builtin_amdgcn_s_sleep(2);
      const int off = WS_QX + (g * 1024 + lane) * 16;
      for (;;) {
        asm volatile("" ::: "memory");
#pragma unroll
        for (int i = 0; i < 16; ++i) wr[i] = __builtin_amdgcn_raw_buffer_load_b128(rs, off + (i & 3) * 1024, (i >> 2) * 4096, AUX_SC1);
        u32 bad = 0u;
#pragma unroll
        for (int i = 0; i < 16; ++i) bad |= rec_bad(wr[i], t1, t2);
        if (__all(bad == 0u)) break;
        if (now() - t0 > SPIN_CAP) break;
        __builtin_amdgcn_s_sleep(8);
      }
#pragma unroll
      for (int i = 0; i < 4; ++i) {
        const float a = (__uint_as_float(wr[i].x) + __uint_as_float(wr[4 + i].x)) + (__uint_as_float(wr[8 + i].x) + __uint_as_float(wr[12 + i].x));
        const float b = (__uint_as_float(wr[i].z) + __uint_as_float(wr[4 + i].z)) + (__uint_as_float(wr[8 + i].z) + __uint_as_float(wr[12 + i].z));
        *(u32*)&lds[L_QF + i * 64 + lane] = pack_bf16(a, b);
      }
    }
    asm volatile("s_waitcnt lgkmcnt(0)" ::: "memory");
    if (lane == 0) cw_store(lds, CW_QRDY, 1u);
    u32 tre;
    {
      cw_wait<8>(lds, CW_NML, NW);
      const f32x2 ml = *(const f32x2*)&lds[L_ML + ((lane >> 4) * NW + (lane & 7)) * 2];
      float M = ml.x;
      M = fmaxf(M, dppf<XOR1>(M)); M = fmaxf(M, dppf<XOR2>(M)); M = fmaxf(M, dppf<ROR4>(M));
      float L = ml.y * e2(ml.x - M);
      L += dppf<XOR1>(L); L += dppf<XOR2>(L); L += dppf<ROR4>(L);
      if ((lane & 15) == 0) st_sc1(rs, WS_ML + ((g * 5 + (lane >> 4)) * 32 + j) * 16, rec_make(__float_as_uint(M), __float_as_uint(L), t1, t2));
      const u64 tm = now();
      if (lane == 1) st_sc1(rs, WS_ML + ((g * 5 + 4) * 32 + j) * 16, rec_make((u32)tm, 0u, t1, t2));
      while (now() - tm < T_ML) __builtin_amdgcn_s_sleep(8);
      const int off = WS_ML + ((g * 5 + (lane >> 4)) * 32 + (lane & 15)) * 16;
      v4u r0, r1, r2;
      for (;;) {
        asm volatile("" ::: "memory");
        r2 = ld_sc1(rs, WS_ML + ((g * 5 + 4) * 32 + (lane & 31)) * 16);
        r0 = ld_sc1(rs, off); r1 = ld_sc1(rs, off + 256);
        if (__all((rec_bad(r0, t1, t2) | rec_bad(r1, t1, t2) | rec_bad(r2, t1, t2)) == 0u)) break;
        if (now() - tm > SPIN_CAP) break;
        __builtin_amdgcn_s_sleep(32);
      }
      float dt = (float)(int)(r2.x - (u32)tm);
      dt += dppf<XOR1>(dt); dt += dppf<XOR2>(dt); dt += dppf<ROR4>(dt); dt += dppf<ROR8>(dt); dt = xrow_sum(dt);
      tre = __builtin_amdgcn_readfirstlane((u32)tm + (u32)((int)(dt * (1.0f / 64.0f)) + G_C - g_due(18)));
      if (lane == 0) cw_store(lds, CW_TG, tre);
      float Mg = fmaxf(__uint_as_float(r0.x), __uint_as_float(r1.x));
      Mg = fmaxf(Mg, dppf<XOR1>(Mg)); Mg = fmaxf(Mg, dppf<XOR2>(Mg)); Mg = fmaxf(Mg, dppf<ROR4>(Mg)); Mg = fmaxf(Mg, dppf<ROR8>(Mg));
      float Lg = __uint_as_float(r0.z) * e2(__uint_as_float(r0.x) - Mg) + __uint_as_float(r1.z) * e2(__uint_as_float(r1.x) - Mg);
      Lg += dppf<XOR1>(Lg); Lg += dppf<XOR2>(Lg); Lg += dppf<ROR4>(Lg); Lg += dppf<ROR8>(Lg);
      if ((lane & 8) == 0) lds[L_F + (lane >> 4) * NW + (lane & 7)] = e2(ml.x - Mg) / Lg;
    }
    cw_wait<2>(lds, CW_NLAST, NW);
    {
      const __amdgpu_buffer_rsrc_t ro = __builtin_amdgcn_make_buffer_rsrc((void*)(wo + (size_t)(g * 512 + j * 16) * DM), 0, 16 * DM * 2, 0x00020000);
#pragma unroll
      for (int r = 0; r < 16; ++r) wr[r] = __builtin_amdgcn_raw_buffer_load_b128(ro, lane * 16 + (r & 3) * 1024, (r >> 2) * 4096, AUX_NT);
      FENCE();
    }
    wg_barrier();
    u32 wp[8][8];
#pragma unroll
    for (int r = 0; r < 8; ++r) {
#pragma unroll
      for (int e = 0; e < 4; ++e) { wp[r][2 * e] = lo16s(wr[r][e], wr[r + 8][e]); wp[r][2 * e + 1] = hi16s(wr[r][e], wr[r + 8][e]); }
      asm volatile("" : "+v"(wp[r][0]), "+v"(wp[r][1]), "+v"(wp[r][2]), "+v"(wp[r][3]), "+v"(wp[r][4]), "+v"(wp[r][5]), "+v"(wp[r][6]), "+v"(wp[r][7]));
    }
    FENCE();
    int offv = WS_P1 + ((g * 32 + j) * 32 + (lane >> 3) * 4) * 128 + (lane & 7) * 16;
    int p2off = WS_P2 + ((lane >> 3) * 256 + bid) * 128 + (lane & 7) * 16;
    asm volatile("" : "+v"(offv), "+v"(p2off));
    __builtin_amdgcn_s_sleep(10);
    for (int i = 0; i < 256 && (int)((u32)now() - tre) < g_due(32) + G_TL; ++i) __builtin_amdgcn_s_sleep(1);
    const f32x2 o = gather_sum(rs, offv, t1, t2);
    const u32 opk = pack_bf16(o.x, o.y);
    u32 so[8];
#pragma unroll
    for (int r = 0; r < 8; ++r) so[r] = __builtin_amdgcn_readlane(opk, r);
    FENCE();
    float y[8];
#pragma unroll
    for (int c = 0; c < 8; ++c) y[c] = 0.f;
#pragma unroll
    for (int r = 0; r < 8; ++r)
#pragma unroll
      for (int c = 0; c < 8; ++c) y[c] = dot2(wp[r][c], so[r], y[c]);
#pragma unroll
    for (int i = 0; i < 4; ++i)
      __builtin_amdgcn_raw_buffer_store_b128(rec_make(__float_as_uint(y[2 * i]), __float_as_uint(y[2 * i + 1]), t1, t2), rs, p2off, i * (8 * 256 * 128), AUX_SC1);
    FENCE();
    if (is_fin) {
      wg_barrier();
      wg_barrier();
      if (lane < 8) {
        f32x2 v[NW];
#pragma unroll
        for (int i = 0; i < NW; ++i) v[i] = *(const f32x2*)&lds[L_FIN + i * 16 + lane * 2];
        const f32x2 s = ((v[0] + v[1]) + (v[2] + v[3])) + ((v[4] + v[5]) + (v[6] + v[7]));
        *(GAS u32*)(out + (fin & 7) * 64 + lane * 8 + (fin >> 3) * 2) = pack_bf16(s.x, s.y);
        if (bid == 0 && lane == 0) __hip_atomic_store((GAS u64*)(ws + WS_EPOCH), ep + 1ull, __ATOMIC_RELAXED, __HIP_MEMORY_SCOPE_AGENT);
      }
    }
  }
}

extern "C" void kernel_launch(void* const* d_in, const int* in_sizes, int n_in, void* d_out, int out_size, void* d_ws, size_t ws_size,
                              hipStream_t stream) {
  gqa_decode_kernel<<<NWG, NTHR, 0, stream>>>((const u16*)d_in[0], (const u16*)d_in[1], (const u16*)d_in[2], (const u16*)d_in[3],
                                              (const u16*)d_in[4], (u16*)d_out, (unsigned char*)d_ws);
}
```

```cpp
#include <hip/hip_runtime.h>
#include <hip/hip_bf16.h>
#include <stdint.h>

typedef unsigned short u16;
typedef unsigned int u32;
typedef unsigned long long u64;
typedef u32 v4u __attribute__((ext_vector_type(4)));
typedef float f32x4 __attribute__((ext_vector_type(4)));
typedef float f32x2 __attribute__((ext_vector_type(2)));
typedef __bf16 bf16x8 __attribute__((ext_vector_type(8)));
typedef __bf16 bf16x2 __attribute__((ext_vector_type(2)));
#define GAS __attribute__((address_space(1)))
#define LAS __attribute__((address_space(3)))

constexpr int DM = 512, HQ = 32, SK = 65536, DH = 128;
constexpr int NWG = 256, NW = 8, NTHR = (NW + 1) * 64;
constexpr int NBLK = SK / 32 / 32 / NW;
constexpr float C2 = 0.088388347648318440f * 1.4426950408889634f;

#define KV_PAT "axwwwyypxqrxcb"
constexpr int pat_stride(char ch, int idx) {
  for (int i = 0; i < 14; ++i) if (KV_PAT[i] == ch && idx-- == 0) return 1024 << i;
  return 0;
}
constexpr int toff(int t) { return (t & 1) * pat_stride('a', 0) + ((t >> 1) & 1) * pat_stride('b', 0) + (t >> 2) * pat_stride('c', 0); }
__device__ __forceinline__ int noff(int n) { return (n & 1) * pat_stride('p', 0) + ((n >> 1) & 1) * pat_stride('q', 0) + (n >> 2) * pat_stride('r', 0); }

constexpr int G_S0 = 145, G_PER = 215;
constexpr int G_D2 = 90;
constexpr int G_C = 75;
constexpr int G_TL = 117, G_T2 = 92;
constexpr int T_Q = 600, T_ML = 300;
constexpr int g_due(int hb) { return G_S0 + ((hb * G_PER) >> 1); }
constexpr u64 SPIN_CAP = 20000000ull;

constexpr int WS_EPOCH = 0;
constexpr int WS_ML = 4096;
constexpr int WS_QX = 32768;
constexpr int WS_P1 = 262144;
constexpr int WS_P2 = WS_P1 + (1 << 20);
constexpr int WS_END = WS_P2 + (1 << 20);

constexpr int PROW = 132, POFF = 544;
constexpr int WREG = POFF + 512;
constexpr int L_W = 0;
constexpr int L_ML = L_W + NW * WREG;
constexpr int L_F = L_ML + NW * 8;
constexpr int L_QF = L_F + NW * 4;
constexpr int L_CTL = L_QF + 256;
constexpr int L_FIN = L_CTL + 8;
constexpr int L_END = L_FIN + NW * 8;
constexpr int CW_NML = 0, CW_NLAST = 1, CW_QRDY = 2, CW_TG = 3, CW_T1 = 4, CW_T2 = 5;

__device__ __forceinline__ f32x4 mfma16(v4u a, v4u b, f32x4 c) {
  return __builtin_amdgcn_mfma_f32_16x16x32_bf16(__builtin_bit_cast(bf16x8, a), __builtin_bit_cast(bf16x8, b), c, 0, 0, 0);
}
__device__ __forceinline__ u32 pack_bf16(float lo, float hi) {
  bf16x2 r; r.x = (__bf16)lo; r.y = (__bf16)hi; return __builtin_bit_cast(u32, r);
}
__device__ __forceinline__ float dot2(u32 a, u32 b, float c) {
  return __builtin_amdgcn_fdot2_f32_bf16(__builtin_bit_cast(bf16x2, a), __builtin_bit_cast(bf16x2, b), c, false);
}
__device__ __forceinline__ u32 lo16s(u32 a, u32 b) { return __builtin_amdgcn_perm(b, a, 0x05040100u); }
__device__ __forceinline__ u32 hi16s(u32 a, u32 b) { return __builtin_amdgcn_perm(b, a, 0x07060302u); }
template <int CTRL> __device__ __forceinline__ float dppf(float x) {
  return __builtin_bit_cast(float, __builtin_amdgcn_mov_dpp(__builtin_bit_cast(int, x), CTRL, 0xf, 0xf, true));
}
template <int BANK> __device__ __forceinline__ float bank_put(float old, float src) {
  return __builtin_bit_cast(float, __builtin_amdgcn_update_dpp(__builtin_bit_cast(int, old), __builtin_bit_cast(int, src), 0xE4, 0xf, 1 << BANK, false));
}
constexpr int ROR4 = 0x124, ROR8 = 0x128, ROR12 = 0x12C, XOR1 = 0xB1, XOR2 = 0x4E;
__device__ __forceinline__ float xrow_max(float x) {
  auto s = __builtin_amdgcn_permlane16_swap(__float_as_uint(x), __float_as_uint(x), false, false);
  x = fmaxf(__uint_as_float(s[0]), __uint_as_float(s[1]));
  auto t = __builtin_amdgcn_permlane32_swap(__float_as_uint(x), __float_as_uint(x), false, false);
  return fmaxf(__uint_as_float(t[0]), __uint_as_float(t[1]));
}
__device__ __forceinline__ float xrow_sum(float x) {
  auto s = __builtin_amdgcn_permlane16_swap(__float_as_uint(x), __float_as_uint(x), false, false);
  x = __uint_as_float(s[0]) + __uint_as_float(s[1]);
  auto t = __builtin_amdgcn_permlane32_swap(__float_as_uint(x), __float_as_uint(x), false, false);
  return __uint_as_float(t[0]) + __uint_as_float(t[1]);
}
__device__ __forceinline__ float e2(float x) { return __builtin_amdgcn_exp2f(x); }
__device__ __forceinline__ u64 now() { return __builtin_amdgcn_s_memrealtime(); }
__device__ __forceinline__ void wg_barrier() {
  asm volatile("s_waitcnt lgkmcnt(0)" ::: "memory");
  __builtin_amdgcn_s_barrier();
  asm volatile("" ::: "memory");
}
#define FENCE() __builtin_amdgcn_sched_barrier(0)
__device__ __forceinline__ u32 cw_load(float* lds, int i) { return __hip_atomic_load((u32*)&lds[L_CTL + i], __ATOMIC_RELAXED, __HIP_MEMORY_SCOPE_WORKGROUP); }
__device__ __forceinline__ void cw_store(float* lds, int i, u32 v) { __hip_atomic_store((u32*)&lds[L_CTL + i], v, __ATOMIC_RELAXED, __HIP_MEMORY_SCOPE_WORKGROUP); }
__device__ __forceinline__ void cw_inc(float* lds, int i) { __hip_atomic_fetch_add((u32*)&lds[L_CTL + i], 1u, __ATOMIC_RELAXED, __HIP_MEMORY_SCOPE_WORKGROUP); }
template <int SLEEP> __device__ __forceinline__ void cw_wait(float* lds, int i, u32 want) {
  const u64 tb = now();
  while (cw_load(lds, i) < want) {
    if (now() - tb > 2 * SPIN_CAP) break;
    __builtin_amdgcn_s_sleep(SLEEP);
  }
  asm volatile("" ::: "memory");
}

__device__ __forceinline__ v4u rec_make(u32 a, u32 b, u32 t1, u32 t2) { return v4u{a, t1 ^ b, b, t2 ^ a}; }
__device__ __forceinline__ u32 rec_bad(v4u r, u32 t1, u32 t2) { return (r.y ^ r.z ^ t1) | (r.w ^ r.x ^ t2); }
constexpr int AUX_NT = 2, AUX_SC1 = 16;
__device__ __forceinline__ v4u ld_sc1(__amdgpu_buffer_rsrc_t rs, int off) { return __builtin_amdgcn_raw_buffer_load_b128(rs, off, 0, AUX_SC1); }
__device__ __forceinline__ void st_sc1(__amdgpu_buffer_rsrc_t rs, int off, v4u v) { __builtin_amdgcn_raw_buffer_store_b128(v, rs, off, 0, AUX_SC1); }
template <int N, int STRIDE> __device__ __forceinline__ f32x2 gather_sum(__amdgpu_buffer_rsrc_t rs, int off, u32 t1, u32 t2) {
  static_assert(N == 2 || N == 4, "");
  const u64 tb = now();
  v4u r[N];
  bool ok = false;
  for (;;) {
    asm volatile("" ::: "memory");
    if (!ok) {
#pragma unroll
      for (int i = 0; i < N; ++i) r[i] = ld_sc1(rs, off + i * STRIDE);
      u32 bad = 0u;
#pragma unroll
      for (int i = 0; i < N; ++i) bad |= rec_bad(r[i], t1, t2);
      ok = bad == 0u;
    }
    if (__all(ok)) break;
    if (now() - tb > SPIN_CAP) break;
  }
  float a = __uint_as_float(r[0].x) + __uint_as_float(r[1].x), b = __uint_as_float(r[0].z) + __uint_as_float(r[1].z);
  if constexpr (N == 4) { a += __uint_as_float(r[2].x) + __uint_as_float(r[3].x); b += __uint_as_float(r[2].z) + __uint_as_float(r[3].z); }
  else { a += dppf<ROR4>(a); b += dppf<ROR4>(b); }
  a += dppf<ROR8>(a); b += dppf<ROR8>(b);
  return f32x2{xrow_sum(a), xrow_sum(b)};
}
__device__ __forceinline__ void wait_until(u32 base, int ticks) {
  for (int i = 0; i < 256 && (int)((u32)now() - base) < ticks; ++i) __builtin_amdgcn_s_sleep(1);
}

__global__ void __launch_bounds__(NTHR, 3) gqa_decode_kernel(const u16* __restrict__ x, const u16* __restrict__ kc, const u16* __restrict__ vc,
                                                             const u16* __restrict__ wq, const u16* __restrict__ wo, u16* __restrict__ out,
                                                             unsigned char* ws) {
  __shared__ __attribute__((aligned(16))) float lds[L_END];
  asm volatile("" :: "s"(x), "s"(kc), "s"(vc), "s"(wq), "s"(wo), "s"(out), "s"(ws));
  const int tid = threadIdx.x, lane = tid & 63, wv = __builtin_amdgcn_readfirstlane(tid >> 6);
  const int bid = blockIdx.x, g = bid >> 5, j = bid & 31;
  const bool is_fin = (bid & 3) == ((bid >> 3) & 3);
  const int fin = bid >> 2;
  const __amdgpu_buffer_rsrc_t rs = __builtin_amdgcn_make_buffer_rsrc((void*)ws, 0, WS_END, 0x00020000);

  if (wv > 0) {
    const __amdgpu_buffer_rsrc_t rk = __builtin_amdgcn_make_buffer_rsrc((void*)(kc + (size_t)g * (SK * DH)), 0, SK * DH * 2, 0x00020000);
    const __amdgpu_buffer_rsrc_t rv = __builtin_amdgcn_make_buffer_rsrc((void*)(vc + (size_t)g * (SK * DH)), 0, SK * DH * 2, 0x00020000);
    const int w = wv - 1, g4 = lane >> 4;
    const int kl = ((lane >> 2) & 3) * 256 + g4 * 64 + (lane & 3) * 16, vl = lane * 16;
    const int sb = (j & 1) * pat_stride('x', 0) + ((j >> 1) & 1) * pat_stride('x', 1) + ((j >> 2) & 1) * pat_stride('x', 2) + ((j >> 3) & 1) * pat_stride('y', 0) +
                   (j >> 4) * pat_stride('y', 1) + (w & 1) * pat_stride('w', 0) + ((w >> 1) & 1) * pat_stride('w', 1) + (w >> 2) * pat_stride('w', 2);
#define LDK(n, t) __builtin_amdgcn_raw_buffer_load_b128(rk, kl, sb + noff(n) + toff(t), AUX_NT)
#define LDV(n, t) __builtin_amdgcn_raw_buffer_load_b128(rv, vl, sb + noff(n) + toff(t), AUX_NT)
    u32 tg = (u32)now();
    int lead = 0;
    const int gd2 = G_D2;
#define PACE(blk, t) do {                                                                                                                  \
      if (((t) & 3) == 0) { lead = g_due(2 * (blk) + ((t) >> 2)) - (int)((u32)now() - tg); FENCE(); }                                      \
      if ((blk) >= 3)                                                                                                                      \
        asm volatile("s_waitcnt vmcnt(3)\n\ts_cmp_gt_i32 %0, 0\n\ts_cbranch_scc0 1f\n\ts_waitcnt vmcnt(2)\n\ts_cmp_gt_i32 %0, %1\n\t"      \
                     "s_cbranch_scc0 1f\n\ts_waitcnt vmcnt(1)\n1:" :: "s"(lead), "s"(gd2) : "memory", "scc");                              \
      else                                                                                                                                 \
        asm volatile("s_waitcnt vmcnt(3)\n\ts_cmp_gt_i32 %0, 0\n\ts_cbranch_scc0 1f\n\ts_waitcnt vmcnt(2)\n1:" :: "s"(lead) : "memory", "scc"); \
      FENCE(); } while (0)
    v4u A[8], B[8], C[8], D[8];
    float* const wreg = &lds[L_W + w * WREG];
#pragma unroll
    for (int t = 0; t < 8; ++t) {
      if (t >= 4) { asm volatile("s_waitcnt vmcnt(3)" ::: "memory"); FENCE(); }
      A[t] = LDK(0, t); FENCE();
      if (t == 3) { wg_barrier(); FENCE(); }
    }
#pragma unroll
    for (int t = 0; t < 8; ++t) { PACE(1, t); B[t] = LDK(1, t); FENCE(); }
#pragma unroll
    for (int t = 0; t < 8; ++t) { PACE(2, t); C[t] = LDK(2, t); FENCE(); }
#pragma unroll
    for (int t = 0; t < 8; ++t) { PACE(3, t); D[t] = LDK(3, t); FENCE(); }
    cw_wait<1>(lds, CW_QRDY, 1u);
    const v4u qf = *(const v4u*)&lds[L_QF + (lane & 3) * 64 + g4 * 16 + ((lane >> 2) & 3) * 4];
    float sc[NBLK][2];

#define QKH(KK, n, G) do {                                                                                  \
      f32x4 d[4];                                                                                           \
      _Pragma("unroll") for (int t = 0; t < 4; ++t) d[t] = mfma16(KK[4 * (G) + t], qf, f32x4{0.f, 0.f, 0.f, 0.f}); \
      float T[4];                                                                                           \
      _Pragma("unroll") for (int r = 0; r < 4; ++r) {                                                       \
        float tt = d[r][0];                                                                                 \
        tt = bank_put<1>(tt, d[(1 + r) & 3][1]);                                                            \
        tt = bank_put<2>(tt, d[(2 + r) & 3][2]);                                                            \
        tt = bank_put<3>(tt, d[(3 + r) & 3][3]);                                                            \
        T[r] = tt; }                                                                                        \
      sc[n][G] = (T[0] + dppf<ROR4>(T[1])) + (dppf<ROR8>(T[2]) + dppf<ROR12>(T[3]));                        \
      asm volatile("" : "+v"(sc[n][G]));                                                                    \
      FENCE();                                                                                              \
    } while (0)
#define PV(VV, n) do {                                                                                      \
      const v4u P = *(const v4u*)(wreg + POFF + (n) * 64 + g4 * 16 + (lane & 3) * 4);                       \
      _Pragma("unroll") for (int e = 0; e < 4; ++e) {                                                       \
        const v4u lo = {lo16s(VV[0][e], VV[4][e]), lo16s(VV[1][e], VV[5][e]), lo16s(VV[2][e], VV[6][e]), lo16s(VV[3][e], VV[7][e])}; \
        const v4u hi = {hi16s(VV[0][e], VV[4][e]), hi16s(VV[1][e], VV[5][e]), hi16s(VV[2][e], VV[6][e]), hi16s(VV[3][e], VV[7][e])}; \
        acc[2 * e] = mfma16(lo, P, acc[2 * e]);                                                             \
        acc[2 * e + 1] = mfma16(hi, P, acc[2 * e + 1]); }                                                   \
      FENCE();                                                                                              \
    } while (0)
#define STEP(X, nx, Y, ny) _Pragma("unroll") for (int t = 0; t < 8; ++t) { PACE(nx, t); X[t] = LDK(nx, t); FENCE(); if (t == 2) QKH(Y, ny, 0); if (t == 6) QKH(Y, ny, 1); }

    QKH(A, 0, 0);
#pragma unroll
    for (int t = 0; t < 8; ++t) {
      PACE(4, t); A[t] = LDK(4, t); FENCE();
      if (t == 0) QKH(A, 0, 1);
      if (t == 1) QKH(B, 1, 0);
      if (t == 2) QKH(B, 1, 1);
      if (t == 3) QKH(C, 2, 0);
      if (t == 4) QKH(C, 2, 1);
      if (t == 5) QKH(D, 3, 0);
      if (t == 6) QKH(D, 3, 1);
    }
    STEP(B, 5, A, 4)
    STEP(A, 6, B, 5)
    STEP(B, 7, A, 6)
#pragma unroll
    for (int t = 0; t < 8; ++t) {
      PACE(8, t); A[t] = LDV(0, t); FENCE();
      if (t == 2) QKH(B, 7, 0);
      if (t == 4) QKH(B, 7, 1);
      if (t == 5) {
        float mx = fmaxf(sc[0][0], sc[0][1]);
#pragma unroll
        for (int n = 1; n < NBLK; ++n) mx = fmaxf(mx, fmaxf(sc[n][0], sc[n][1]));
        mx = fmaxf(mx, dppf<ROR4>(mx)); mx = fmaxf(mx, dppf<ROR8>(mx)); mx = xrow_max(mx);
        const float m = mx * C2;
        float l = 0.f;
#pragma unroll
        for (int n = 0; n < NBLK; ++n) {
          const float p0 = e2(fmaf(sc[n][0], C2, -m)), p1 = e2(fmaf(sc[n][1], C2, -m));
          l += p0 + p1;
          *(u32*)(wreg + POFF + n * 64 + g4 * 16 + (lane & 3) * 4 + ((lane >> 2) & 3)) = pack_bf16(p0, p1);
        }
        l += dppf<ROR4>(l); l += dppf<ROR8>(l); l = xrow_sum(l);
        if (lane < 4) *(f32x2*)&lds[L_ML + (lane * NW + w) * 2] = f32x2{m, l};
        asm volatile("s_waitcnt lgkmcnt(0)" ::: "memory");
        if (lane == 0) cw_inc(lds, CW_NML);
        FENCE();
      }
    }
    f32x4 acc[8];
#pragma unroll
    for (int i = 0; i < 8; ++i) acc[i] = f32x4{0.f, 0.f, 0.f, 0.f};
#pragma unroll 1
    for (int n = 1; n < NBLK - 1; n += 2) {
#pragma unroll
      for (int t = 0; t < 8; ++t) {
        PACE(8 + n, t); B[t] = LDV(n, t); FENCE();
        if (t == 6) { tg = __builtin_amdgcn_readfirstlane(cw_load(lds, CW_TG)); PV(A, n - 1); }
      }
#pragma unroll
      for (int t = 0; t < 8; ++t) {
        PACE(9 + n, t); A[t] = LDV(n + 1, t); FENCE();
        if (t == 6) { tg = __builtin_amdgcn_readfirstlane(cw_load(lds, CW_TG)); PV(B, n); }
      }
    }
#pragma unroll
    for (int t = 0; t < 8; ++t) {
      PACE(15, t); B[t] = LDV(NBLK - 1, t); FENCE();
      if (t == 6) PV(A, NBLK - 2);
    }
    if (lane == 0) cw_inc(lds, CW_NLAST);
    FENCE();
    const u32 t1 = cw_load(lds, CW_T1), t2 = cw_load(lds, CW_T2);
    int p1off = WS_P1 + ((g * 32 + (w & 3) * 8 + g4 * 2 + (lane & 1)) * 32 + j) * 128 + ((lane >> 1) & 7) * 16;
    LAS f32x4* rowp = (LAS f32x4*)(wreg + (lane & 3) * PROW + 32 * g4);
    const LAS f32x2* rdp = (const LAS f32x2*)&lds[L_W + (w & 3) * PROW + 2 * lane];
    const LAS f32x4* fp = (const LAS f32x4*)&lds[L_F + (w & 3) * NW];
    {
      v4u P = *(const v4u*)(wreg + POFF + (NBLK - 1) * 64 + g4 * 16 + (lane & 3) * 4);
      asm volatile("" : "+v"(P), "+v"(p1off), "+v"(rowp), "+v"(rdp), "+v"(fp));
      v4u fr[8];
#pragma unroll
      for (int u = 0; u < 3; ++u) {
#pragma unroll
        for (int e = 0; e < 4; ++e) { fr[2 * e][u] = lo16s(B[u][e], B[u + 4][e]); fr[2 * e + 1][u] = hi16s(B[u][e], B[u + 4][e]); }
        asm volatile("" : "+v"(fr[0][u]), "+v"(fr[1][u]), "+v"(fr[2][u]), "+v"(fr[3][u]), "+v"(fr[4][u]), "+v"(fr[5][u]), "+v"(fr[6][u]), "+v"(fr[7][u]));
        FENCE();
      }
#pragma unroll
      for (int e = 0; e < 4; ++e) { fr[2 * e][3] = lo16s(B[3][e], B[7][e]); fr[2 * e + 1][3] = hi16s(B[3][e], B[7][e]); }
#pragma unroll
      for (int i = 0; i < 8; ++i) acc[i] = mfma16(fr[i], P, acc[i]);
      FENCE();
#pragma unroll
      for (int i = 0; i < 8; ++i) rowp[i] = acc[i];
    }
    wg_barrier();
    if (w < 4) {
      f32x2 pv[NW];
#pragma unroll
      for (int i = 0; i < NW; ++i) pv[i] = rdp[i * (WREG / 2)];
      const f32x4 fa = fp[0], fb = fp[1];
      const f32x2 o = ((pv[0] * fa[0] + pv[1] * fa[1]) + (pv[2] * fa[2] + pv[3] * fa[3])) + ((pv[4] * fb[0] + pv[5] * fb[1]) + (pv[6] * fb[2] + pv[7] * fb[3]));
      st_sc1(rs, p1off, rec_make(__float_as_uint(o.x), __float_as_uint(o.y), t1, t2));
    }
    if (is_fin) {
      int off = WS_P2 + fin * 16384 + w * 2048 + lane * 16;
      asm volatile("" : "+v"(off));
      wg_barrier();
      __builtin_amdgcn_s_sleep(4);
      wait_until(tg, g_due(32) + G_TL + G_T2);
      const f32x2 s = gather_sum<2, 1024>(rs, off, t1, t2);
      if (lane < 4) *(f32x2*)&lds[L_FIN + w * 8 + lane * 2] = s;
      wg_barrier();
    }
  } else {
    __builtin_amdgcn_s_setprio(3);
    const u64 t0 = now();
    if (lane == 0) *(v4u*)&lds[L_CTL] = v4u{0u, 0u, 0u, (u32)t0};
    wg_barrier();
    FENCE();
    v4u wr[16], xr[2];
    {
      const __amdgpu_buffer_rsrc_t rq = __builtin_amdgcn_make_buffer_rsrc((void*)wq, 0, DM * HQ * DH * 2, 0x00020000);
      const int row0 = (j & 3) * 128 + (lane >> 3) * 16;
      const int vo = row0 * (HQ * DH * 2) + g * 1024 + (j >> 2) * 128 + (lane & 7) * 16;
#pragma unroll
      for (int r = 0; r < 16; ++r) wr[r] = __builtin_amdgcn_raw_buffer_load_b128(rq, vo, r * (HQ * DH * 2), AUX_NT);
      xr[0] = *(const GAS v4u*)(x + row0); xr[1] = *(const GAS v4u*)(x + row0 + 8);
    }
    const u64 ep = __hip_atomic_load((GAS u64*)(ws + WS_EPOCH), __ATOMIC_RELAXED, __HIP_MEMORY_SCOPE_AGENT);
    FENCE();
    float qv[8];
#pragma unroll
    for (int c = 0; c < 8; ++c) qv[c] = 0.f;
#pragma unroll
    for (int r = 0; r < 8; ++r)
#pragma unroll
      for (int e = 0; e < 4; ++e) {
        qv[2 * e] = dot2(lo16s(wr[2 * r][e], wr[2 * r + 1][e]), xr[r >> 2][r & 3], qv[2 * e]);
        qv[2 * e + 1] = dot2(hi16s(wr[2 * r][e], wr[2 * r + 1][e]), xr[r >> 2][r & 3], qv[2 * e + 1]);
      }
    float q4[4];
#pragma unroll
    for (int c = 0; c < 4; ++c) {
      const float a = qv[c] + dppf<ROR8>(qv[c]), b = qv[c + 4] + dppf<ROR8>(qv[c + 4]);
      q4[c] = xrow_sum((lane & 8) ? b : a);
    }
    u64 z = ep + 0x9E3779B97F4A7C15ull;
    z = (z ^ (z >> 30)) * 0xBF58476D1CE4E5B9ull; z = (z ^ (z >> 27)) * 0x94D049BB133111EBull; z ^= z >> 31;
    const u32 t1 = __builtin_amdgcn_readfirstlane((u32)z) | 1u, t2 = __builtin_amdgcn_readfirstlane((u32)(z >> 32));
    if (lane < 32)
      st_sc1(rs, WS_QX + ((g * 4 + (j & 3)) * 256 + (j >> 2) * 32 + (lane & 7) * 4 + ((lane >> 3) & 1) * 2 + (lane >> 4)) * 16,
             rec_make(__float_as_uint((lane & 16) ? q4[2] : q4[0]), __float_as_uint((lane & 16) ? q4[3] : q4[1]), t1, t2));
    if (lane == 0) { cw_store(lds, CW_T1, t1); cw_store(lds, CW_T2, t2); }
    {
      while (now() - t0 < T_Q) __builtin_amdgcn_s_sleep(2);
      const int off = WS_QX + (g * 1024 + lane) * 16;
      for (;;) {
        asm volatile("" ::: "memory");
#pragma unroll
        for (int i = 0; i < 16; ++i) wr[i] = __builtin_amdgcn_raw_buffer_load_b128(rs, off + (i & 3) * 1024, (i >> 2) * 4096, AUX_SC1);
        u32 bad = 0u;
#pragma unroll
        for (int i = 0; i < 16; ++i) bad |= rec_bad(wr[i], t1, t2);
        if (__all(bad == 0u)) break;
        if (now() - t0 > SPIN_CAP) break;
        __builtin_amdgcn_s_sleep(8);
      }
#pragma unroll
      for (int i = 0; i < 4; ++i) {
        const float a = (__uint_as_float(wr[i].x) + __uint_as_float(wr[4 + i].x)) + (__uint_as_float(wr[8 + i].x) + __uint_as_float(wr[12 + i].x));
        const float b = (__uint_as_float(wr[i].z) + __uint_as_float(wr[4 + i].z)) + (__uint_as_float(wr[8 + i].z) + __uint_as_float(wr[12 + i].z));
        *(u32*)&lds[L_QF + i * 64 + lane] = pack_bf16(a, b);
      }
    }
    asm volatile("s_waitcnt lgkmcnt(0)" ::: "memory");
    if (lane == 0) cw_store(lds, CW_QRDY, 1u);
    u32 tre;
    {
      cw_wait<8>(lds, CW_NML, NW);
      const f32x2 ml = *(const f32x2*)&lds[L_ML + ((lane >> 4) * NW + (lane & 7)) * 2];
      float M = ml.x;
      M = fmaxf(M, dppf<XOR1>(M)); M = fmaxf(M, dppf<XOR2>(M)); M = fmaxf(M, dppf<ROR4>(M));
      float L = ml.y * e2(ml.x - M);
      L += dppf<XOR1>(L); L += dppf<XOR2>(L); L += dppf<ROR4>(L);
      if ((lane & 15) == 0) st_sc1(rs, WS_ML + ((g * 5 + (lane >> 4)) * 32 + j) * 16, rec_make(__float_as_uint(M), __float_as_uint(L), t1, t2));
      const u64 tm = now();
      if (lane == 1) st_sc1(rs, WS_ML + ((g * 5 + 4) * 32 + j) * 16, rec_make((u32)tm, 0u, t1, t2));
      while (now() - tm < T_ML) __builtin_amdgcn_s_sleep(8);
      const int off = WS_ML + ((g * 5 + (lane >> 4)) * 32 + (lane & 15)) * 16;
      v4u r0, r1, r2;
      for (;;) {
        asm volatile("" ::: "memory");
        r2 = ld_sc1(rs, WS_ML + ((g * 5 + 4) * 32 + (lane & 31)) * 16);
        r0 = ld_sc1(rs, off); r1 = ld_sc1(rs, off + 256);
        if (__all((rec_bad(r0, t1, t2) | rec_bad(r1, t1, t2) | rec_bad(r2, t1, t2)) == 0u)) break;
        if (now() - tm > SPIN_CAP) break;
        __builtin_amdgcn_s_sleep(32);
      }
      float dt = (float)(int)(r2.x - (u32)tm);
      dt += dppf<XOR1>(dt); dt += dppf<XOR2>(dt); dt += dppf<ROR4>(dt); dt += dppf<ROR8>(dt); dt = xrow_sum(dt);
      tre = __builtin_amdgcn_readfirstlane((u32)tm + (u32)((int)(dt * (1.0f / 64.0f)) + G_C - g_due(18)));
      if (lane == 0) cw_store(lds, CW_TG, tre);
      float Mg = fmaxf(__uint_as_float(r0.x), __uint_as_float(r1.x));
      Mg = fmaxf(Mg, dppf<XOR1>(Mg)); Mg = fmaxf(Mg, dppf<XOR2>(Mg)); Mg = fmaxf(Mg, dppf<ROR4>(Mg)); Mg = fmaxf(Mg, dppf<ROR8>(Mg));
      float Lg = __uint_as_float(r0.z) * e2(__uint_as_float(r0.x) - Mg) + __uint_as_float(r1.z) * e2(__uint_as_float(r1.x) - Mg);
      Lg += dppf<XOR1>(Lg); Lg += dppf<XOR2>(Lg); Lg += dppf<ROR4>(Lg); Lg += dppf<ROR8>(Lg);
      if ((lane & 8) == 0) lds[L_F + (lane >> 4) * NW + (lane & 7)] = e2(ml.x - Mg) / Lg;
    }
    cw_wait<2>(lds, CW_NLAST, NW);
    {
      const __amdgpu_buffer_rsrc_t ro = __builtin_amdgcn_make_buffer_rsrc((void*)(wo + (size_t)(g * 512 + j * 16) * DM), 0, 16 * DM * 2, 0x00020000);
#pragma unroll
      for (int r = 0; r < 16; ++r) wr[r] = __builtin_amdgcn_raw_buffer_load_b128(ro, lane * 16 + (r & 3) * 1024, (r >> 2) * 4096, AUX_NT);
      FENCE();
    }
    wg_barrier();
    u32 wp[8][8];
#pragma unroll
    for (int r = 0; r < 8; ++r) {
#pragma unroll
      for (int e = 0; e < 4; ++e) { wp[r][2 * e] = lo16s(wr[r][e], wr[r + 8][e]); wp[r][2 * e + 1] = hi16s(wr[r][e], wr[r + 8][e]); }
      asm volatile("" : "+v"(wp[r][0]), "+v"(wp[r][1]), "+v"(wp[r][2]), "+v"(wp[r][3]), "+v"(wp[r][4]), "+v"(wp[r][5]), "+v"(wp[r][6]), "+v"(wp[r][7]));
    }
    FENCE();
    int offv = WS_P1 + ((g * 32 + j) * 32 + (lane >> 3) * 4) * 128 + (lane & 7) * 16;
    int p2off = WS_P2 + ((lane >> 2) * 256 + bid) * 64 + (lane & 3) * 16;
    asm volatile("" : "+v"(offv), "+v"(p2off));
    __builtin_amdgcn_s_sleep(10);
    wait_until(tre, g_due(32) + G_TL);
    const f32x2 o = gather_sum<4, 128>(rs, offv, t1, t2);
    const u32 opk = pack_bf16(o.x, o.y);
    u32 so[8];
#pragma unroll
    for (int r = 0; r < 8; ++r) so[r] = __builtin_amdgcn_readlane(opk, r);
    FENCE();
    float y[8];
#pragma unroll
    for (int c = 0; c < 8; ++c) y[c] = 0.f;
#pragma unroll
    for (int r = 0; r < 8; ++r)
#pragma unroll
      for (int c = 0; c < 8; ++c) y[c] = dot2(wp[r][c], so[r], y[c]);
#pragma unroll
    for (int i = 0; i < 4; ++i)
      __builtin_amdgcn_raw_buffer_store_b128(rec_make(__float_as_uint(y[2 * i]), __float_as_uint(y[2 * i + 1]), t1, t2), rs, p2off, i * (16 * 256 * 64), AUX_SC1);
    FENCE();
    if (is_fin) {
      wg_barrier();
      wg_barrier();
      if (lane < 4) {
        f32x2 v[NW];
#pragma unroll
        for (int i = 0; i < NW; ++i) v[i] = *(const f32x2*)&lds[L_FIN + i * 8 + lane * 2];
        const f32x2 s = ((v[0] + v[1]) + (v[2] + v[3])) + ((v[4] + v[5]) + (v[6] + v[7]));
        *(GAS u32*)(out + (fin & 15) * 32 + lane * 8 + (fin >> 4) * 2) = pack_bf16(s.x, s.y);
        if (bid == 0 && lane == 0) __hip_atomic_store((GAS u64*)(ws + WS_EPOCH), ep + 1ull, __ATOMIC_RELAXED, __HIP_MEMORY_SCOPE_AGENT);
      }
    }
  }
}

extern "C" void kernel_launch(void* const* d_in, const int* in_sizes, int n_in, void* d_out, int out_size, void* d_ws, size_t ws_size,
                              hipStream_t stream) {
  gqa_decode_kernel<<<NWG, NTHR, 0, stream>>>((const u16*)d_in[0], (const u16*)d_in[1], (const u16*)d_in[2], (const u16*)d_in[3],
                                              (const u16*)d_in[4], (u16*)d_out, (unsigned char*)d_ws);
}
```

```cpp
#define SYNC_OFF 0
#include <hip/hip_runtime.h>
#include <hip/hip_bf16.h>
#include <stdint.h>

typedef unsigned short u16;
typedef unsigned int u32;
typedef unsigned long long u64;
typedef u32 v4u __attribute__((ext_vector_type(4)));
typedef float f32x4 __attribute__((ext_vector_type(4)));
typedef float f32x2 __attribute__((ext_vector_type(2)));
typedef __bf16 bf16x8 __attribute__((ext_vector_type(8)));
typedef __bf16 bf16x2 __attribute__((ext_vector_type(2)));
#define GAS __attribute__((address_space(1)))
#define LAS __attribute__((address_space(3)))

constexpr int DM = 512, HQ = 32, SK = 65536, DH = 128;
constexpr int NWG = 256, NW = 8, NTHR = (NW + 1) * 64;
constexpr int NBLK = SK / 32 / 32 / NW;
constexpr float C2 = 0.088388347648318440f * 1.4426950408889634f;

#define KV_PAT "axwwwyypxqrxcb"
constexpr int pat_stride(char ch, int idx) {
  for (int i = 0; i < 14; ++i) if (KV_PAT[i] == ch && idx-- == 0) return 1024 << i;
  return 0;
}
constexpr int toff(int t) { return (t & 1) * pat_stride('a', 0) + ((t >> 1) & 1) * pat_stride('b', 0) + (t >> 2) * pat_stride('c', 0); }
__device__ __forceinline__ int noff(int n) { return (n & 1) * pat_stride('p', 0) + ((n >> 1) & 1) * pat_stride('q', 0) + (n >> 2) * pat_stride('r', 0); }

constexpr int G_S0 = 145, G_PER = 215;
constexpr int G_D2 = 90;
constexpr int G_C = 75;
constexpr int G_TL = 117, G_T2 = 92;
constexpr int T_Q = 600, T_ML = 300;
constexpr int g_due(int hb) { return G_S0 + ((hb * G_PER) >> 1); }
constexpr u64 SPIN_CAP = 20000000ull;

constexpr int WS_EPOCH = 0;
constexpr int WS_ML = 4096;
constexpr int WS_TS = 28672;
constexpr int WS_QX = 32768;
constexpr int WS_P1 = 262144;
constexpr int WS_P2 = WS_P1 + (1 << 20);
constexpr int WS_END = WS_P2 + (1 << 20);

constexpr int PROW = 132, POFF = 544;
constexpr int WREG = POFF + 512;
constexpr int L_W = 0;
constexpr int L_ML = L_W + NW * WREG;
constexpr int L_F = L_ML + NW * 8;
constexpr int L_QF = L_F + NW * 4;
constexpr int L_CTL = L_QF + 256;
constexpr int L_FIN = L_CTL + 8;
constexpr int L_END = L_FIN + NW * 8;
constexpr int CW_NML = 0, CW_NLAST = 1, CW_QRDY = 2, CW_TG = 3, CW_T1 = 4, CW_T2 = 5;

__device__ __forceinline__ f32x4 mfma16(v4u a, v4u b, f32x4 c) {
  return __builtin_amdgcn_mfma_f32_16x16x32_bf16(__builtin_bit_cast(bf16x8, a), __builtin_bit_cast(bf16x8, b), c, 0, 0, 0);
}
__device__ __forceinline__ u32 pack_bf16(float lo, float hi) {
  bf16x2 r; r.x = (__bf16)lo; r.y = (__bf16)hi; return __builtin_bit_cast(u32, r);
}
__device__ __forceinline__ float dot2(u32 a, u32 b, float c) {
  return __builtin_amdgcn_fdot2_f32_bf16(__builtin_bit_cast(bf16x2, a), __builtin_bit_cast(bf16x2, b), c, false);
}
__device__ __forceinline__ u32 lo16s(u32 a, u32 b) { return __builtin_amdgcn_perm(b, a, 0x05040100u); }
__device__ __forceinline__ u32 hi16s(u32 a, u32 b) { return __builtin_amdgcn_perm(b, a, 0x07060302u); }
template <int CTRL> __device__ __forceinline__ float dppf(float x) {
  return __builtin_bit_cast(float, __builtin_amdgcn_mov_dpp(__builtin_bit_cast(int, x), CTRL, 0xf, 0xf, true));
}
template <int BANK> __device__ __forceinline__ float bank_put(float old, float src) {
  return __builtin_bit_cast(float, __builtin_amdgcn_update_dpp(__builtin_bit_cast(int, old), __builtin_bit_cast(int, src), 0xE4, 0xf, 1 << BANK, false));
}
constexpr int ROR4 = 0x124, ROR8 = 0x128, ROR12 = 0x12C, XOR1 = 0xB1, XOR2 = 0x4E;
__device__ __forceinline__ float xrow_max(float x) {
  auto s = __builtin_amdgcn_permlane16_swap(__float_as_uint(x), __float_as_uint(x), false, false);
  x = fmaxf(__uint_as_float(s[0]), __uint_as_float(s[1]));
  auto t = __builtin_amdgcn_permlane32_swap(__float_as_uint(x), __float_as_uint(x), false, false);
  return fmaxf(__uint_as_float(t[0]), __uint_as_float(t[1]));
}
__device__ __forceinline__ float xrow_sum(float x) {
  auto s = __builtin_amdgcn_permlane16_swap(__float_as_uint(x), __float_as_uint(x), false, false);
  x = __uint_as_float(s[0]) + __uint_as_float(s[1]);
  auto t = __builtin_amdgcn_permlane32_swap(__float_as_uint(x), __float_as_uint(x), false, false);
  return __uint_as_float(t[0]) + __uint_as_float(t[1]);
}
__device__ __forceinline__ float e2(float x) { return __builtin_amdgcn_exp2f(x); }
__device__ __forceinline__ u64 now() { return __builtin_amdgcn_s_memrealtime(); }
__device__ __forceinline__ void wg_barrier() {
  asm volatile("s_waitcnt lgkmcnt(0)" ::: "memory");
  __builtin_amdgcn_s_barrier();
  asm volatile("" ::: "memory");
}
#define FENCE() __builtin_amdgcn_sched_barrier(0)
__device__ __forceinline__ u32 cw_load(float* lds, int i) { return __hip_atomic_load((u32*)&lds[L_CTL + i], __ATOMIC_RELAXED, __HIP_MEMORY_SCOPE_WORKGROUP); }
__device__ __forceinline__ void cw_store(float* lds, int i, u32 v) { __hip_atomic_store((u32*)&lds[L_CTL + i], v, __ATOMIC_RELAXED, __HIP_MEMORY_SCOPE_WORKGROUP); }
__device__ __forceinline__ void cw_inc(float* lds, int i) { __hip_atomic_fetch_add((u32*)&lds[L_CTL + i], 1u, __ATOMIC_RELAXED, __HIP_MEMORY_SCOPE_WORKGROUP); }
template <int SLEEP> __device__ __forceinline__ void cw_wait(float* lds, int i, u32 want) {
  const u64 tb = now();
  while (cw_load(lds, i) < want) {
    if (now() - tb > 2 * SPIN_CAP) break;
    __builtin_amdgcn_s_sleep(SLEEP);
  }
  asm volatile("" ::: "memory");
}

__device__ __forceinline__ v4u rec_make(u32 a, u32 b, u32 t1, u32 t2) { return v4u{a, t1 ^ b, b, t2 ^ a}; }
__device__ __forceinline__ u32 rec_bad(v4u r, u32 t1, u32 t2) { return (r.y ^ r.z ^ t1) | (r.w ^ r.x ^ t2); }
constexpr int AUX_NT = 2, AUX_SC1 = 16;
__device__ __forceinline__ v4u ld_sc1(__amdgpu_buffer_rsrc_t rs, int off) { return __builtin_amdgcn_raw_buffer_load_b128(rs, off, 0, AUX_SC1); }
__device__ __forceinline__ void st_sc1(__amdgpu_buffer_rsrc_t rs, int off, v4u v) { __builtin_amdgcn_raw_buffer_store_b128(v, rs, off, 0, AUX_SC1); }
template <int N, int STRIDE> __device__ __forceinline__ f32x2 gather_sum(__amdgpu_buffer_rsrc_t rs, int off, u32 t1, u32 t2) {
  static_assert(N == 2 || N == 4, "");
  const u64 tb = now();
  v4u r[N];
  bool ok = false;
  for (;;) {
    asm volatile("" ::: "memory");
    if (!ok) {
#pragma unroll
      for (int i = 0; i < N; ++i) r[i] = ld_sc1(rs, off + i * STRIDE);
      u32 bad = 0u;
#pragma unroll
      for (int i = 0; i < N; ++i) bad |= rec_bad(r[i], t1, t2);
      ok = bad == 0u;
    }
    if (__all(ok)) break;
    if (now() - tb > SPIN_CAP) break;
  }
  float a = __uint_as_float(r[0].x) + __uint_as_float(r[1].x), b = __uint_as_float(r[0].z) + __uint_as_float(r[1].z);
  if constexpr (N == 4) { a += __uint_as_float(r[2].x) + __uint_as_float(r[3].x); b += __uint_as_float(r[2].z) + __uint_as_float(r[3].z); }
  else { a += dppf<ROR4>(a); b += dppf<ROR4>(b); }
  a += dppf<ROR8>(a); b += dppf<ROR8>(b);
  return f32x2{xrow_sum(a), xrow_sum(b)};
}
__device__ __forceinline__ void wait_until(u32 base, int ticks) {
  for (int i = 0; i < 256 && (int)((u32)now() - base) < ticks; ++i) __builtin_amdgcn_s_sleep(1);
}

__global__ void __launch_bounds__(NTHR, 3) gqa_decode_kernel(const u16* __restrict__ x, const u16* __restrict__ kc, const u16* __restrict__ vc,
                                                             const u16* __restrict__ wq, const u16* __restrict__ wo, u16* __restrict__ out,
                                                             unsigned char* ws) {
  __shared__ __attribute__((aligned(16))) float lds[L_END];
  asm volatile("" :: "s"(x), "s"(kc), "s"(vc), "s"(wq), "s"(wo), "s"(out), "s"(ws));
  const int tid = threadIdx.x, lane = tid & 63, wv = __builtin_amdgcn_readfirstlane(tid >> 6);
  const int bid = blockIdx.x, g = bid >> 5, j = bid & 31;
  const bool is_fin = (bid & 3) == ((bid >> 3) & 3);
  const int fin = bid >> 2;
  const __amdgpu_buffer_rsrc_t rs = __builtin_amdgcn_make_buffer_rsrc((void*)ws, 0, WS_END, 0x00020000);

  if (wv > 0) {
    const __amdgpu_buffer_rsrc_t rk = __builtin_amdgcn_make_buffer_rsrc((void*)(kc + (size_t)g * (SK * DH)), 0, SK * DH * 2, 0x00020000);
    const __amdgpu_buffer_rsrc_t rv = __builtin_amdgcn_make_buffer_rsrc((void*)(vc + (size_t)g * (SK * DH)), 0, SK * DH * 2, 0x00020000);
    const int w = wv - 1, g4 = lane >> 4;
    const int kl = ((lane >> 2) & 3) * 256 + g4 * 64 + (lane & 3) * 16, vl = lane * 16;
    const int sb = (j & 1) * pat_stride('x', 0) + ((j >> 1) & 1) * pat_stride('x', 1) + ((j >> 2) & 1) * pat_stride('x', 2) + ((j >> 3) & 1) * pat_stride('y', 0) +
                   (j >> 4) * pat_stride('y', 1) + (w & 1) * pat_stride('w', 0) + ((w >> 1) & 1) * pat_stride('w', 1) + (w >> 2) * pat_stride('w', 2);
#define LDK(n, t) __builtin_amdgcn_raw_buffer_load_b128(rk, kl, sb + noff(n) + toff(t), AUX_NT)
#define LDV(n, t) __builtin_amdgcn_raw_buffer_load_b128(rv, vl, sb + noff(n) + toff(t), AUX_NT)
    u32 tg = (u32)now();
    int lead = 0;
    const int gd2 = G_D2;
#define PACE(blk, t) do {                                                                                                                  \
      if (((t) & 3) == 0) { lead = g_due(2 * (blk) + ((t) >> 2)) - (int)((u32)now() - tg); FENCE(); }                                      \
      if ((blk) >= 3)                                                                                                                      \
        asm volatile("s_waitcnt vmcnt(3)\n\ts_cmp_gt_i32 %0, 0\n\ts_cbranch_scc0 1f\n\ts_waitcnt vmcnt(2)\n\ts_cmp_gt_i32 %0, %1\n\t"      \
                     "s_cbranch_scc0 1f\n\ts_waitcnt vmcnt(1)\n1:" :: "s"(lead), "s"(gd2) : "memory", "scc");                              \
      else                                                                                                                                 \
        asm volatile("s_waitcnt vmcnt(3)\n\ts_cmp_gt_i32 %0, 0\n\ts_cbranch_scc0 1f\n\ts_waitcnt vmcnt(2)\n1:" :: "s"(lead) : "memory", "scc"); \
      FENCE(); } while (0)
    v4u A[8], B[8], C[8], D[8];
    float* const wreg = &lds[L_W + w * WREG];
#pragma unroll
    for (int t = 0; t < 8; ++t) {
      if (t >= 4) { asm volatile("s_waitcnt vmcnt(3)" ::: "memory"); FENCE(); }
      A[t] = LDK(0, t); FENCE();
      if (t == 3) { wg_barrier(); FENCE(); }
    }
#pragma unroll
    for (int t = 0; t < 8; ++t) { PACE(1, t); B[t] = LDK(1, t); FENCE(); }
#pragma unroll
    for (int t = 0; t < 8; ++t) { PACE(2, t); C[t] = LDK(2, t); FENCE(); }
#pragma unroll
    for (int t = 0; t < 8; ++t) { PACE(3, t); D[t] = LDK(3, t); FENCE(); }
    cw_wait<1>(lds, CW_QRDY, 1u);
    const v4u qf = *(const v4u*)&lds[L_QF + (lane & 3) * 64 + g4 * 16 + ((lane >> 2) & 3) * 4];
    tg = __builtin_amdgcn_readfirstlane(cw_load(lds, CW_TG));
    float sc[NBLK][2];

#define QKH(KK, n, G) do {                                                                                  \
      f32x4 d[4];                                                                                           \
      _Pragma("unroll") for (int t = 0; t < 4; ++t) d[t] = mfma16(KK[4 * (G) + t], qf, f32x4{0.f, 0.f, 0.f, 0.f}); \
      float T[4];                                                                                           \
      _Pragma("unroll") for (int r = 0; r < 4; ++r) {                                                       \
        float tt = d[r][0];                                                                                 \
        tt = bank_put<1>(tt, d[(1 + r) & 3][1]);                                                            \
        tt = bank_put<2>(tt, d[(2 + r) & 3][2]);                                                            \
        tt = bank_put<3>(tt, d[(3 + r) & 3][3]);                                                            \
        T[r] = tt; }                                                                                        \
      sc[n][G] = (T[0] + dppf<ROR4>(T[1])) + (dppf<ROR8>(T[2]) + dppf<ROR12>(T[3]));                        \
      asm volatile("" : "+v"(sc[n][G]));                                                                    \
      FENCE();                                                                                              \
    } while (0)
#define PV(VV, n) do {                                                                                      \
      const v4u P = *(const v4u*)(wreg + POFF + (n) * 64 + g4 * 16 + (lane & 3) * 4);                       \
      _Pragma("unroll") for (int e = 0; e < 4; ++e) {                                                       \
        const v4u lo = {lo16s(VV[0][e], VV[4][e]), lo16s(VV[1][e], VV[5][e]), lo16s(VV[2][e], VV[6][e]), lo16s(VV[3][e], VV[7][e])}; \
        const v4u hi = {hi16s(VV[0][e], VV[4][e]), hi16s(VV[1][e], VV[5][e]), hi16s(VV[2][e], VV[6][e]), hi16s(VV[3][e], VV[7][e])}; \
        acc[2 * e] = mfma16(lo, P, acc[2 * e]);                                                             \
        acc[2 * e + 1] = mfma16(hi, P, acc[2 * e + 1]); }                                                   \
      FENCE();                                                                                              \
    } while (0)
#define STEP(X, nx, Y, ny) _Pragma("unroll") for (int t = 0; t < 8; ++t) { PACE(nx, t); X[t] = LDK(nx, t); FENCE(); if (t == 2) QKH(Y, ny, 0); if (t == 6) QKH(Y, ny, 1); }

    QKH(A, 0, 0);
#pragma unroll
    for (int t = 0; t < 8; ++t) {
      PACE(4, t); A[t] = LDK(4, t); FENCE();
      if (t == 0) QKH(A, 0, 1);
      if (t == 1) QKH(B, 1, 0);
      if (t == 2) QKH(B, 1, 1);
      if (t == 3) QKH(C, 2, 0);
      if (t == 4) QKH(C, 2, 1);
      if (t == 5) QKH(D, 3, 0);
      if (t == 6) QKH(D, 3, 1);
    }
    STEP(B, 5, A, 4)
    STEP(A, 6, B, 5)
    STEP(B, 7, A, 6)
#pragma unroll
    for (int t = 0; t < 8; ++t) {
      PACE(8, t); A[t] = LDV(0, t); FENCE();
      if (t == 2) QKH(B, 7, 0);
      if (t == 4) QKH(B, 7, 1);
      if (t == 5) {
        float mx = fmaxf(sc[0][0], sc[0][1]);
#pragma unroll
        for (int n = 1; n < NBLK; ++n) mx = fmaxf(mx, fmaxf(sc[n][0], sc[n][1]));
        mx = fmaxf(mx, dppf<ROR4>(mx)); mx = fmaxf(mx, dppf<ROR8>(mx)); mx = xrow_max(mx);
        const float m = mx * C2;
        float l = 0.f;
#pragma unroll
        for (int n = 0; n < NBLK; ++n) {
          const float p0 = e2(fmaf(sc[n][0], C2, -m)), p1 = e2(fmaf(sc[n][1], C2, -m));
          l += p0 + p1;
          *(u32*)(wreg + POFF + n * 64 + g4 * 16 + (lane & 3) * 4 + ((lane >> 2) & 3)) = pack_bf16(p0, p1);
        }
        l += dppf<ROR4>(l); l += dppf<ROR8>(l); l = xrow_sum(l);
        if (lane < 4) *(f32x2*)&lds[L_ML + (lane * NW + w) * 2] = f32x2{m, l};
        asm volatile("s_waitcnt lgkmcnt(0)" ::: "memory");
        if (lane == 0) cw_inc(lds, CW_NML);
        FENCE();
      }
    }
    f32x4 acc[8];
#pragma unroll
    for (int i = 0; i < 8; ++i) acc[i] = f32x4{0.f, 0.f, 0.f, 0.f};
#pragma unroll 1
    for (int n = 1; n < NBLK - 1; n += 2) {
#pragma unroll
      for (int t = 0; t < 8; ++t) {
        PACE(8 + n, t); B[t] = LDV(n, t); FENCE();
        if (t == 6) { tg = __builtin_amdgcn_readfirstlane(cw_load(lds, CW_TG)); PV(A, n - 1); }
      }
#pragma unroll
      for (int t = 0; t < 8; ++t) {
        PACE(9 + n, t); A[t] = LDV(n + 1, t); FENCE();
        if (t == 6) { tg = __builtin_amdgcn_readfirstlane(cw_load(lds, CW_TG)); PV(B, n); }
      }
    }
#pragma unroll
    for (int t = 0; t < 8; ++t) {
      PACE(15, t); B[t] = LDV(NBLK - 1, t); FENCE();
      if (t == 6) PV(A, NBLK - 2);
    }
    if (lane == 0) cw_inc(lds, CW_NLAST);
    FENCE();
    const u32 t1 = cw_load(lds, CW_T1), t2 = cw_load(lds, CW_T2);
    int p1off = WS_P1 + ((g * 32 + (w & 3) * 8 + g4 * 2 + (lane & 1)) * 32 + j) * 128 + ((lane >> 1) & 7) * 16;
    LAS f32x4* rowp = (LAS f32x4*)(wreg + (lane & 3) * PROW + 32 * g4);
    const LAS f32x2* rdp = (const LAS f32x2*)&lds[L_W + (w & 3) * PROW + 2 * lane];
    const LAS f32x4* fp = (const LAS f32x4*)&lds[L_F + (w & 3) * NW];
    {
      v4u P = *(const v4u*)(wreg + POFF + (NBLK - 1) * 64 + g4 * 16 + (lane & 3) * 4);
      asm volatile("" : "+v"(P), "+v"(p1off), "+v"(rowp), "+v"(rdp), "+v"(fp));
      v4u fr[8];
#pragma unroll
      for (int u = 0; u < 3; ++u) {
#pragma unroll
        for (int e = 0; e < 4; ++e) { fr[2 * e][u] = lo16s(B[u][e], B[u + 4][e]); fr[2 * e + 1][u] = hi16s(B[u][e], B[u + 4][e]); }
        asm volatile("" : "+v"(fr[0][u]), "+v"(fr[1][u]), "+v"(fr[2][u]), "+v"(fr[3][u]), "+v"(fr[4][u]), "+v"(fr[5][u]), "+v"(fr[6][u]), "+v"(fr[7][u]));
        FENCE();
      }
#pragma unroll
      for (int e = 0; e < 4; ++e) { fr[2 * e][3] = lo16s(B[3][e], B[7][e]); fr[2 * e + 1][3] = hi16s(B[3][e], B[7][e]); }
#pragma unroll
      for (int i = 0; i < 8; ++i) acc[i] = mfma16(fr[i], P, acc[i]);
      FENCE();
#pragma unroll
      for (int i = 0; i < 8; ++i) rowp[i] = acc[i];
    }
    wg_barrier();
    if (w < 4) {
      f32x2 pv[NW];
#pragma unroll
      for (int i = 0; i < NW; ++i) pv[i] = rdp[i * (WREG / 2)];
      const f32x4 fa = fp[0], fb = fp[1];
      const f32x2 o = ((pv[0] * fa[0] + pv[1] * fa[1]) + (pv[2] * fa[2] + pv[3] * fa[3])) + ((pv[4] * fb[0] + pv[5] * fb[1]) + (pv[6] * fb[2] + pv[7] * fb[3]));
      st_sc1(rs, p1off, rec_make(__float_as_uint(o.x), __float_as_uint(o.y), t1, t2));
    }
    if (is_fin) {
      int off = WS_P2 + fin * 16384 + w * 2048 + lane * 16;
      asm volatile("" : "+v"(off));
      wg_barrier();
      __builtin_amdgcn_s_sleep(4);
      wait_until(tg, g_due(32) + G_TL + G_T2);
      const f32x2 s = gather_sum<2, 1024>(rs, off, t1, t2);
      if (lane < 4) *(f32x2*)&lds[L_FIN + w * 8 + lane * 2] = s;
      wg_barrier();
    }
  } else {
    __builtin_amdgcn_s_setprio(3);
    const u64 t0 = now();
    if (lane == 0) *(v4u*)&lds[L_CTL] = v4u{0u, 0u, 0u, (u32)t0};
    wg_barrier();
    FENCE();
    u32 spread = 0;
    v4u wr[16], xr[2];
    {
      const __amdgpu_buffer_rsrc_t rq = __builtin_amdgcn_make_buffer_rsrc((void*)wq, 0, DM * HQ * DH * 2, 0x00020000);
      const int row0 = (j & 3) * 128 + (lane >> 3) * 16;
      const int vo = row0 * (HQ * DH * 2) + g * 1024 + (j >> 2) * 128 + (lane & 7) * 16;
#pragma unroll
      for (int r = 0; r < 16; ++r) wr[r] = __builtin_amdgcn_raw_buffer_load_b128(rq, vo, r * (HQ * DH * 2), AUX_NT);
      xr[0] = *(const GAS v4u*)(x + row0); xr[1] = *(const GAS v4u*)(x + row0 + 8);
    }
    const u64 ep = __hip_atomic_load((GAS u64*)(ws + WS_EPOCH), __ATOMIC_RELAXED, __HIP_MEMORY_SCOPE_AGENT);
    FENCE();
    float qv[8];
#pragma unroll
    for (int c = 0; c < 8; ++c) qv[c] = 0.f;
#pragma unroll
    for (int r = 0; r < 8; ++r)
#pragma unroll
      for (int e = 0; e < 4; ++e) {
        qv[2 * e] = dot2(lo16s(wr[2 * r][e], wr[2 * r + 1][e]), xr[r >> 2][r & 3], qv[2 * e]);
        qv[2 * e + 1] = dot2(hi16s(wr[2 * r][e], wr[2 * r + 1][e]), xr[r >> 2][r & 3], qv[2 * e + 1]);
      }
    float q4[4];
#pragma unroll
    for (int c = 0; c < 4; ++c) {
      const float a = qv[c] + dppf<ROR8>(qv[c]), b = qv[c + 4] + dppf<ROR8>(qv[c + 4]);
      q4[c] = xrow_sum((lane & 8) ? b : a);
    }
    u64 z = ep + 0x9E3779B97F4A7C15ull;
    z = (z ^ (z >> 30)) * 0xBF58476D1CE4E5B9ull; z = (z ^ (z >> 27)) * 0x94D049BB133111EBull; z ^= z >> 31;
    const u32 t1 = __builtin_amdgcn_readfirstlane((u32)z) | 1u, t2 = __builtin_amdgcn_readfirstlane((u32)(z >> 32));
    if (lane < 32)
      st_sc1(rs, WS_QX + ((g * 4 + (j & 3)) * 256 + (j >> 2) * 32 + (lane & 7) * 4 + ((lane >> 3) & 1) * 2 + (lane >> 4)) * 16,
             rec_make(__float_as_uint((lane & 16) ? q4[2] : q4[0]), __float_as_uint((lane & 16) ? q4[3] : q4[1]), t1, t2));
    if (lane == 32) st_sc1(rs, WS_TS + (g * 32 + j) * 16, rec_make((u32)t0, 0u, t1, t2));
    if (lane == 0) { cw_store(lds, CW_T1, t1); cw_store(lds, CW_T2, t2); }
    {
      while (now() - t0 < T_Q) __builtin_amdgcn_s_sleep(2);
      const int off = WS_QX + (g * 1024 + lane) * 16;
      v4u rt;
      for (;;) {
        asm volatile("" ::: "memory");
#pragma unroll
        for (int i = 0; i < 16; ++i) wr[i] = __builtin_amdgcn_raw_buffer_load_b128(rs, off + (i & 3) * 1024, (i >> 2) * 4096, AUX_SC1);
        rt = ld_sc1(rs, WS_TS + (g * 32 + (lane & 31)) * 16);
        u32 bad = rec_bad(rt, t1, t2);
#pragma unroll
        for (int i = 0; i < 16; ++i) bad |= rec_bad(wr[i], t1, t2);
        if (__all(bad == 0u)) break;
        if (now() - t0 > SPIN_CAP) break;
        __builtin_amdgcn_s_sleep(8);
      }
#pragma unroll
      for (int i = 0; i < 4; ++i) {
        const float a = (__uint_as_float(wr[i].x) + __uint_as_float(wr[4 + i].x)) + (__uint_as_float(wr[8 + i].x) + __uint_as_float(wr[12 + i].x));
        const float b = (__uint_as_float(wr[i].z) + __uint_as_float(wr[4 + i].z)) + (__uint_as_float(wr[8 + i].z) + __uint_as_float(wr[12 + i].z));
        *(u32*)&lds[L_QF + i * 64 + lane] = pack_bf16(a, b);
      }
      float dt = (float)(int)(rt.x - (u32)t0);
      dt += dppf<XOR1>(dt); dt += dppf<XOR2>(dt); dt += dppf<ROR4>(dt); dt += dppf<ROR8>(dt); dt = xrow_sum(dt);
      if (lane == 0) cw_store(lds, CW_TG, (u32)t0 + (u32)((int)(dt * (1.0f / 64.0f)) - SYNC_OFF));
    }
    asm volatile("s_waitcnt lgkmcnt(0)" ::: "memory");
    if (lane == 0) cw_store(lds, CW_QRDY, 1u);
    u32 tre;
    {
      cw_wait<8>(lds, CW_NML, NW);
      const f32x2 ml = *(const f32x2*)&lds[L_ML + ((lane >> 4) * NW + (lane & 7)) * 2];
      float M = ml.x;
      M = fmaxf(M, dppf<XOR1>(M)); M = fmaxf(M, dppf<XOR2>(M)); M = fmaxf(M, dppf<ROR4>(M));
      float L = ml.y * e2(ml.x - M);
      L += dppf<XOR1>(L); L += dppf<XOR2>(L); L += dppf<ROR4>(L);
      if ((lane & 15) == 0) st_sc1(rs, WS_ML + ((g * 5 + (lane >> 4)) * 32 + j) * 16, rec_make(__float_as_uint(M), __float_as_uint(L), t1, t2));
      const u64 tm = now();
      if (lane == 1) st_sc1(rs, WS_ML + ((g * 5 + 4) * 32 + j) * 16, rec_make((u32)tm, 0u, t1, t2));
      while (now() - tm < T_ML) __builtin_amdgcn_s_sleep(8);
      const int off = WS_ML + ((g * 5 + (lane >> 4)) * 32 + (lane & 15)) * 16;
      v4u r0, r1, r2;
      for (;;) {
        asm volatile("" ::: "memory");
        r2 = ld_sc1(rs, WS_ML + ((g * 5 + 4) * 32 + (lane & 31)) * 16);
        r0 = ld_sc1(rs, off); r1 = ld_sc1(rs, off + 256);
        if (__all((rec_bad(r0, t1, t2) | rec_bad(r1, t1, t2) | rec_bad(r2, t1, t2)) == 0u)) break;
        if (now() - tm > SPIN_CAP) break;
        __builtin_amdgcn_s_sleep(32);
      }
      float dt = (float)(int)(r2.x - (u32)tm);
      dt += dppf<XOR1>(dt); dt += dppf<XOR2>(dt); dt += dppf<ROR4>(dt); dt += dppf<ROR8>(dt); dt = xrow_sum(dt);
      tre = __builtin_amdgcn_readfirstlane((u32)tm + (u32)((int)(dt * (1.0f / 64.0f)) + G_C - g_due(18)));
      if (lane == 0) cw_store(lds, CW_TG, tre);
      float Mg = fmaxf(__uint_as_float(r0.x), __uint_as_float(r1.x));
      Mg = fmaxf(Mg, dppf<XOR1>(Mg)); Mg = fmaxf(Mg, dppf<XOR2>(Mg)); Mg = fmaxf(Mg, dppf<ROR4>(Mg)); Mg = fmaxf(Mg, dppf<ROR8>(Mg));
      float Lg = __uint_as_float(r0.z) * e2(__uint_as_float(r0.x) - Mg) + __uint_as_float(r1.z) * e2(__uint_as_float(r1.x) - Mg);
      Lg += dppf<XOR1>(Lg); Lg += dppf<XOR2>(Lg); Lg += dppf<ROR4>(Lg); Lg += dppf<ROR8>(Lg);
      if ((lane & 8) == 0) lds[L_F + (lane >> 4) * NW + (lane & 7)] = e2(ml.x - Mg) / Lg;
    }
    cw_wait<2>(lds, CW_NLAST, NW);
    {
      const __amdgpu_buffer_rsrc_t ro = __builtin_amdgcn_make_buffer_rsrc((void*)(wo + (size_t)(g * 512 + j * 16) * DM), 0, 16 * DM * 2, 0x00020000);
#pragma unroll
      for (int r = 0; r < 16; ++r) wr[r] = __builtin_amdgcn_raw_buffer_load_b128(ro, lane * 16 + (r & 3) * 1024, (r >> 2) * 4096, AUX_NT);
      FENCE();
    }
    wg_barrier();
    u32 wp[8][8];
#pragma unroll
    for (int r = 0; r < 8; ++r) {
#pragma unroll
      for (int e = 0; e < 4; ++e) { wp[r][2 * e] = lo16s(wr[r][e], wr[r + 8][e]); wp[r][2 * e + 1] = hi16s(wr[r][e], wr[r + 8][e]); }
      asm volatile("" : "+v"(wp[r][0]), "+v"(wp[r][1]), "+v"(wp[r][2]), "+v"(wp[r][3]), "+v"(wp[r][4]), "+v"(wp[r][5]), "+v"(wp[r][6]), "+v"(wp[r][7]));
    }
    FENCE();
    int offv = WS_P1 + ((g * 32 + j) * 32 + (lane >> 3) * 4) * 128 + (lane & 7) * 16;
    int p2off = WS_P2 + ((lane >> 2) * 256 + bid) * 64 + (lane & 3) * 16;
    asm volatile("" : "+v"(offv), "+v"(p2off));
    __builtin_amdgcn_s_sleep(10);
    wait_until(tre, g_due(32) + G_TL);
    const f32x2 o = gather_sum<4, 128>(rs, offv, t1, t2);
    const u32 opk = pack_bf16(o.x, o.y);
    u32 so[8];
#pragma unroll
    for (int r = 0; r < 8; ++r) so[r] = __builtin_amdgcn_readlane(opk, r);
    FENCE();
    float y[8];
#pragma unroll
    for (int c = 0; c < 8; ++c) y[c] = 0.f;
#pragma unroll
    for (int r = 0; r < 8; ++r)
#pragma unroll
      for (int c = 0; c < 8; ++c) y[c] = dot2(wp[r][c], so[r], y[c]);
#pragma unroll
    for (int i = 0; i < 4; ++i)
      __builtin_amdgcn_raw_buffer_store_b128(rec_make(__float_as_uint(y[2 * i]), __float_as_uint(y[2 * i + 1]), t1, t2), rs, p2off, i * (16 * 256 * 64), AUX_SC1);
    FENCE();
    if (is_fin) {
      wg_barrier();
      wg_barrier();
      if (lane < 4) {
        f32x2 v[NW];
#pragma unroll
        for (int i = 0; i < NW; ++i) v[i] = *(const f32x2*)&lds[L_FIN + i * 8 + lane * 2];
        const f32x2 s = ((v[0] + v[1]) + (v[2] + v[3])) + ((v[4] + v[5]) + (v[6] + v[7]));
        *(GAS u32*)(out + (fin & 15) * 32 + lane * 8 + (fin >> 4) * 2) = pack_bf16(s.x, s.y);
        if (bid == 0 && lane == 0) __hip_atomic_store((GAS u64*)(ws + WS_EPOCH), ep + 1ull, __ATOMIC_RELAXED, __HIP_MEMORY_SCOPE_AGENT);
      }
    }
  }
}

extern "C" void kernel_launch(void* const* d_in, const int* in_sizes, int n_in, void* d_out, int out_size, void* d_ws, size_t ws_size,
                              hipStream_t stream) {
  gqa_decode_kernel<<<NWG, NTHR, 0, stream>>>((const u16*)d_in[0], (const u16*)d_in[1], (const u16*)d_in[2], (const u16*)d_in[3],
                                              (const u16*)d_in[4], (u16*)d_out, (unsigned char*)d_ws);
}
```

```cpp
#include <hip/hip_runtime.h>
#include <hip/hip_bf16.h>
#include <stdint.h>

typedef unsigned short u16;
typedef unsigned int u32;
typedef unsigned long long u64;
typedef u32 v4u __attribute__((ext_vector_type(4)));
typedef float f32x4 __attribute__((ext_vector_type(4)));
typedef float f32x2 __attribute__((ext_vector_type(2)));
typedef __bf16 bf16x8 __attribute__((ext_vector_type(8)));
typedef __bf16 bf16x2 __attribute__((ext_vector_type(2)));
#define GAS __attribute__((address_space(1)))
#define LAS __attribute__((address_space(3)))

constexpr int DM = 512, HQ = 32, SK = 65536, DH = 128;
constexpr int NWG = 256, NW = 8, NTHR = (NW + 1) * 64;
constexpr int NBLK = SK / 32 / 32 / NW;
constexpr float C2 = 0.088388347648318440f * 1.4426950408889634f;

#define KV_PAT "axwwwyypxqrxcb"
constexpr int pat_stride(char ch, int idx) {
  for (int i = 0; i < 14; ++i) if (KV_PAT[i] == ch && idx-- == 0) return 1024 << i;
  return 0;
}
constexpr int toff(int t) { return (t & 1) * pat_stride('a', 0) + ((t >> 1) & 1) * pat_stride('b', 0) + (t >> 2) * pat_stride('c', 0); }
__device__ __forceinline__ int noff(int n) { return (n & 1) * pat_stride('p', 0) + ((n >> 1) & 1) * pat_stride('q', 0) + (n >> 2) * pat_stride('r', 0); }

constexpr int G_S0 = 145, G_PER = 215;
constexpr int G_D2 = 90;
constexpr int G_C = 75;
constexpr int G_TL = 117, G_T2 = 92;
constexpr int T_Q = 600, T_ML = 300;
constexpr int g_due(int hb) { return G_S0 + ((hb * G_PER) >> 1); }
constexpr u64 SPIN_CAP = 20000000ull;

constexpr int WS_EPOCH = 0;
constexpr int WS_ML = 4096;
constexpr int WS_QX = 32768;
constexpr int WS_P1 = 262144;
constexpr int WS_P2 = WS_P1 + (1 << 20);
constexpr int WS_END = WS_P2 + (1 << 20);

constexpr int PROW = 132, POFF = 544;
constexpr int WREG = POFF + 512;
constexpr int L_W = 0;
constexpr int L_ML = L_W + NW * WREG;
constexpr int L_F = L_ML + NW * 8;
constexpr int L_QF = L_F + NW * 4;
constexpr int L_CTL = L_QF + 256;
constexpr int L_FIN = L_CTL + 8;
constexpr int L_END = L_FIN + NW * 8;
constexpr int CW_NML = 0, CW_NLAST = 1, CW_QRDY = 2, CW_TG = 3, CW_T1 = 4, CW_T2 = 5;

__device__ __forceinline__ f32x4 mfma16(v4u a, v4u b, f32x4 c) {
  return __builtin_amdgcn_mfma_f32_16x16x32_bf16(__builtin_bit_cast(bf16x8, a), __builtin_bit_cast(bf16x8, b), c, 0, 0, 0);
}
__device__ __forceinline__ u32 pack_bf16(float lo, float hi) {
  bf16x2 r; r.x = (__bf16)lo; r.y = (__bf16)hi; return __builtin_bit_cast(u32, r);
}
__device__ __forceinline__ float dot2(u32 a, u32 b, float c) {
  return __builtin_amdgcn_fdot2_f32_bf16(__builtin_bit_cast(bf16x2, a), __builtin_bit_cast(bf16x2, b), c, false);
}
__device__ __forceinline__ u32 lo16s(u32 a, u32 b) { return __builtin_amdgcn_perm(b, a, 0x05040100u); }
__device__ __forceinline__ u32 hi16s(u32 a, u32 b) { return __builtin_amdgcn_perm(b, a, 0x07060302u); }
template <int CTRL> __device__ __forceinline__ float dppf(float x) {
  return __builtin_bit_cast(float, __builtin_amdgcn_mov_dpp(__builtin_bit_cast(int, x), CTRL, 0xf, 0xf, true));
}
template <int BANK> __device__ __forceinline__ float bank_put(float old, float src) {
  return __builtin_bit_cast(float, __builtin_amdgcn_update_dpp(__builtin_bit_cast(int, old), __builtin_bit_cast(int, src), 0xE4, 0xf, 1 << BANK, false));
}
constexpr int ROR4 = 0x124, ROR8 = 0x128, ROR12 = 0x12C, XOR1 = 0xB1, XOR2 = 0x4E;
__device__ __forceinline__ float xrow_max(float x) {
  auto s = __builtin_amdgcn_permlane16_swap(__float_as_uint(x), __float_as_uint(x), false, false);
  x = fmaxf(__uint_as_float(s[0]), __uint_as_float(s[1]));
  auto t = __builtin_amdgcn_permlane32_swap(__float_as_uint(x), __float_as_uint(x), false, false);
  return fmaxf(__uint_as_float(t[0]), __uint_as_float(t[1]));
}
__device__ __forceinline__ float xrow_sum(float x) {
  auto s = __builtin_amdgcn_permlane16_swap(__float_as_uint(x), __float_as_uint(x), false, false);
  x = __uint_as_float(s[0]) + __uint_as_float(s[1]);
  auto t = __builtin_amdgcn_permlane32_swap(__float_as_uint(x), __float_as_uint(x), false, false);
  return __uint_as_float(t[0]) + __uint_as_float(t[1]);
}
__device__ __forceinline__ float e2(float x) { return __builtin_amdgcn_exp2f(x); }
__device__ __forceinline__ u64 now() { return __builtin_amdgcn_s_memrealtime(); }
__device__ __forceinline__ void wg_barrier() {
  asm volatile("s_waitcnt lgkmcnt(0)" ::: "memory");
  __builtin_amdgcn_s_barrier();
  asm volatile("" ::: "memory");
}
#define FENCE() __builtin_amdgcn_sched_barrier(0)
__device__ __forceinline__ u32 cw_load(float* lds, int i) { return __hip_atomic_load((u32*)&lds[L_CTL + i], __ATOMIC_RELAXED, __HIP_MEMORY_SCOPE_WORKGROUP); }
__device__ __forceinline__ void cw_store(float* lds, int i, u32 v) { __hip_atomic_store((u32*)&lds[L_CTL + i], v, __ATOMIC_RELAXED, __HIP_MEMORY_SCOPE_WORKGROUP); }
__device__ __forceinline__ void cw_inc(float* lds, int i) { __hip_atomic_fetch_add((u32*)&lds[L_CTL + i], 1u, __ATOMIC_RELAXED, __HIP_MEMORY_SCOPE_WORKGROUP); }
template <int SLEEP> __device__ __forceinline__ void cw_wait(float* lds, int i, u32 want) {
  const u64 tb = now();
  while (cw_load(lds, i) < want) {
    if (now() - tb > 2 * SPIN_CAP) break;
    __builtin_amdgcn_s_sleep(SLEEP);
  }
  asm volatile("" ::: "memory");
}

__device__ __forceinline__ v4u rec_make(u32 a, u32 b, u32 t1, u32 t2) { return v4u{a, t1 ^ b, b, t2 ^ a}; }
__device__ __forceinline__ u32 rec_bad(v4u r, u32 t1, u32 t2) { return (r.y ^ r.z ^ t1) | (r.w ^ r.x ^ t2); }
constexpr int AUX_NT = 2, AUX_SC1 = 16;
__device__ __forceinline__ v4u ld_sc1(__amdgpu_buffer_rsrc_t rs, int off) { return __builtin_amdgcn_raw_buffer_load_b128(rs, off, 0, AUX_SC1); }
__device__ __forceinline__ void st_sc1(__amdgpu_buffer_rsrc_t rs, int off, v4u v) { __builtin_amdgcn_raw_buffer_store_b128(v, rs, off, 0, AUX_SC1); }
template <int N, int STRIDE> __device__ __forceinline__ f32x2 gather_sum(__amdgpu_buffer_rsrc_t rs, int off, u32 t1, u32 t2) {
  static_assert(N == 2 || N == 4, "");
  const u64 tb = now();
  v4u r[N];
  bool ok = false;
  for (;;) {
    asm volatile("" ::: "memory");
    if (!ok) {
#pragma unroll
      for (int i = 0; i < N; ++i) r[i] = ld_sc1(rs, off + i * STRIDE);
      u32 bad = 0u;
#pragma unroll
      for (int i = 0; i < N; ++i) bad |= rec_bad(r[i], t1, t2);
      ok = bad == 0u;
    }
    if (__all(ok)) break;
    if (now() - tb > SPIN_CAP) break;
  }
  float a = __uint_as_float(r[0].x) + __uint_as_float(r[1].x), b = __uint_as_float(r[0].z) + __uint_as_float(r[1].z);
  if constexpr (N == 4) { a += __uint_as_float(r[2].x) + __uint_as_float(r[3].x); b += __uint_as_float(r[2].z) + __uint_as_float(r[3].z); }
  else { a += dppf<ROR4>(a); b += dppf<ROR4>(b); }
  a += dppf<ROR8>(a); b += dppf<ROR8>(b);
  return f32x2{xrow_sum(a), xrow_sum(b)};
}
__device__ __forceinline__ void wait_until(u32 base, int ticks) {
  for (int i = 0; i < 256 && (int)((u32)now() - base) < ticks; ++i) __builtin_amdgcn_s_sleep(1);
}

__global__ void __launch_bounds__(NTHR, 3) gqa_decode_kernel(const u16* __restrict__ x, const u16* __restrict__ kc, const u16* __restrict__ vc,
                                                             const u16* __restrict__ wq, const u16* __restrict__ wo, u16* __restrict__ out,
                                                             unsigned char* ws) {
  __shared__ __attribute__((aligned(16))) float lds[L_END];
  const int tid = threadIdx.x, lane = tid & 63, wv = __builtin_amdgcn_readfirstlane(tid >> 6);
  const int bid = blockIdx.x, g = bid >> 5, j = bid & 31;
  const int w = wv - 1, g4 = lane >> 4;
  int kl = ((lane >> 2) & 3) * 256 + g4 * 64 + (lane & 3) * 16;
  int sb = g * (SK * DH * 2) + (j & 1) * pat_stride('x', 0) + ((j >> 1) & 1) * pat_stride('x', 1) + ((j >> 2) & 1) * pat_stride('x', 2) + ((j >> 3) & 1) * pat_stride('y', 0) +
           (j >> 4) * pat_stride('y', 1) + (w & 1) * pat_stride('w', 0) + ((w >> 1) & 1) * pat_stride('w', 1) + ((w >> 2) & 1) * pat_stride('w', 2);
  asm volatile("" : "+v"(kl));
  asm volatile("" : "+s"(sb));
  asm volatile("" :: "s"(x), "s"(kc), "s"(vc), "s"(wq), "s"(wo), "s"(out), "s"(ws));
  const bool is_fin = (bid & 3) == ((bid >> 3) & 3);
  const int fin = bid >> 2;
  const __amdgpu_buffer_rsrc_t rs = __builtin_amdgcn_make_buffer_rsrc((void*)ws, 0, WS_END, 0x00020000);

  if (wv > 0) {
    const __amdgpu_buffer_rsrc_t rk = __builtin_amdgcn_make_buffer_rsrc((void*)kc, 0, 8 * SK * DH * 2, 0x00020000);
    const __amdgpu_buffer_rsrc_t rv = __builtin_amdgcn_make_buffer_rsrc((void*)vc, 0, 8 * SK * DH * 2, 0x00020000);
    const int vl = lane * 16;
#define LDK(n, t) __builtin_amdgcn_raw_buffer_load_b128(rk, kl, sb + noff(n) + toff(t), AUX_NT)
#define LDV(n, t) __builtin_amdgcn_raw_buffer_load_b128(rv, vl, sb + noff(n) + toff(t), AUX_NT)
    u32 tg = (u32)now();
    int lead = 0;
    const int gd2 = G_D2;
#define PACE(blk, t) do {                                                                                                                  \
      if (((t) & 3) == 0) { lead = g_due(2 * (blk) + ((t) >> 2)) - (int)((u32)now() - tg); FENCE(); }                                      \
      if ((blk) >= 3)                                                                                                                      \
        asm volatile("s_waitcnt vmcnt(3)\n\ts_cmp_gt_i32 %0, 0\n\ts_cbranch_scc0 1f\n\ts_waitcnt vmcnt(2)\n\ts_cmp_gt_i32 %0, %1\n\t"      \
                     "s_cbranch_scc0 1f\n\ts_waitcnt vmcnt(1)\n1:" :: "s"(lead), "s"(gd2) : "memory", "scc");                              \
      else                                                                                                                                 \
        asm volatile("s_waitcnt vmcnt(3)\n\ts_cmp_gt_i32 %0, 0\n\ts_cbranch_scc0 1f\n\ts_waitcnt vmcnt(2)\n1:" :: "s"(lead) : "memory", "scc"); \
      FENCE(); } while (0)
    v4u A[8], B[8], C[8], D[8];
    float* const wreg = &lds[L_W + w * WREG];
#pragma unroll
    for (int t = 0; t < 8; ++t) {
      if (t >= 4) { asm volatile("s_waitcnt vmcnt(3)" ::: "memory"); FENCE(); }
      A[t] = LDK(0, t); FENCE();
      if (t == 3) { wg_barrier(); FENCE(); }
    }
#pragma unroll
    for (int t = 0; t < 8; ++t) { PACE(1, t); B[t] = LDK(1, t); FENCE(); }
#pragma unroll
    for (int t = 0; t < 8; ++t) { PACE(2, t); C[t] = LDK(2, t); FENCE(); }
#pragma unroll
    for (int t = 0; t < 8; ++t) { PACE(3, t); D[t] = LDK(3, t); FENCE(); }
    cw_wait<1>(lds, CW_QRDY, 1u);
    const v4u qf = *(const v4u*)&lds[L_QF + (lane & 3) * 64 + g4 * 16 + ((lane >> 2) & 3) * 4];
    float sc[NBLK][2];

#define QKH(KK, n, G) do {                                                                                  \
      f32x4 d[4];                                                                                           \
      _Pragma("unroll") for (int t = 0; t < 4; ++t) d[t] = mfma16(KK[4 * (G) + t], qf, f32x4{0.f, 0.f, 0.f, 0.f}); \
      float T[4];                                                                                           \
      _Pragma("unroll") for (int r = 0; r < 4; ++r) {                                                       \
        float tt = d[r][0];                                                                                 \
        tt = bank_put<1>(tt, d[(1 + r) & 3][1]);                                                            \
        tt = bank_put<2>(tt, d[(2 + r) & 3][2]);                                                            \
        tt = bank_put<3>(tt, d[(3 + r) & 3][3]);                                                            \
        T[r] = tt; }                                                                                        \
      sc[n][G] = (T[0] + dppf<ROR4>(T[1])) + (dppf<ROR8>(T[2]) + dppf<ROR12>(T[3]));                        \
      asm volatile("" : "+v"(sc[n][G]));                                                                    \
      FENCE();                                                                                              \
    } while (0)
#define PV(VV, n) do {                                                                                      \
      const v4u P = *(const v4u*)(wreg + POFF + (n) * 64 + g4 * 16 + (lane & 3) * 4);                       \
      _Pragma("unroll") for (int e = 0; e < 4; ++e) {                                                       \
        const v4u lo = {lo16s(VV[0][e], VV[4][e]), lo16s(VV[1][e], VV[5][e]), lo16s(VV[2][e], VV[6][e]), lo16s(VV[3][e], VV[7][e])}; \
        const v4u hi = {hi16s(VV[0][e], VV[4][e]), hi16s(VV[1][e], VV[5][e]), hi16s(VV[2][e], VV[6][e]), hi16s(VV[3][e], VV[7][e])}; \
        acc[2 * e] = mfma16(lo, P, acc[2 * e]);                                                             \
        acc[2 * e + 1] = mfma16(hi, P, acc[2 * e + 1]); }                                                   \
      FENCE();                                                                                              \
    } while (0)
#define STEP(X, nx, Y, ny) _Pragma("unroll") for (int t = 0; t < 8; ++t) { PACE(nx, t); X[t] = LDK(nx, t); FENCE(); if (t == 2) QKH(Y, ny, 0); if (t == 6) QKH(Y, ny, 1); }

    QKH(A, 0, 0);
#pragma unroll
    for (int t = 0; t < 8; ++t) {
      PACE(4, t); A[t] = LDK(4, t); FENCE();
      if (t == 0) QKH(A, 0, 1);
      if (t == 1) QKH(B, 1, 0);
      if (t == 2) QKH(B, 1, 1);
      if (t == 3) QKH(C, 2, 0);
      if (t == 4) QKH(C, 2, 1);
      if (t == 5) QKH(D, 3, 0);
      if (t == 6) QKH(D, 3, 1);
    }
    STEP(B, 5, A, 4)
    STEP(A, 6, B, 5)
    STEP(B, 7, A, 6)
#pragma unroll
    for (int t = 0; t < 8; ++t) {
      PACE(8, t); A[t] = LDV(0, t); FENCE();
      if (t == 2) QKH(B, 7, 0);
      if (t == 4) QKH(B, 7, 1);
      if (t == 5) {
        float mx = fmaxf(sc[0][0], sc[0][1]);
#pragma unroll
        for (int n = 1; n < NBLK; ++n) mx = fmaxf(mx, fmaxf(sc[n][0], sc[n][1]));
        mx = fmaxf(mx, dppf<ROR4>(mx)); mx = fmaxf(mx, dppf<ROR8>(mx)); mx = xrow_max(mx);
        const float m = mx * C2;
        float l = 0.f;
#pragma unroll
        for (int n = 0; n < NBLK; ++n) {
          const float p0 = e2(fmaf(sc[n][0], C2, -m)), p1 = e2(fmaf(sc[n][1], C2, -m));
          l += p0 + p1;
          *(u32*)(wreg + POFF + n * 64 + g4 * 16 + (lane & 3) * 4 + ((lane >> 2) & 3)) = pack_bf16(p0, p1);
        }
        l += dppf<ROR4>(l); l += dppf<ROR8>(l); l = xrow_sum(l);
        if (lane < 4) *(f32x2*)&lds[L_ML + (lane * NW + w) * 2] = f32x2{m, l};
        asm volatile("s_waitcnt lgkmcnt(0)" ::: "memory");
        if (lane == 0) cw_inc(lds, CW_NML);
        FENCE();
      }
    }
    f32x4 acc[8];
#pragma unroll
    for (int i = 0; i < 8; ++i) acc[i] = f32x4{0.f, 0.f, 0.f, 0.f};
#pragma unroll 1
    for (int n = 1; n < NBLK - 1; n += 2) {
#pragma unroll
      for (int t = 0; t < 8; ++t) {
        PACE(8 + n, t); B[t] = LDV(n, t); FENCE();
        if (t == 6) { tg = __builtin_amdgcn_readfirstlane(cw_load(lds, CW_TG)); PV(A, n - 1); }
      }
#pragma unroll
      for (int t = 0; t < 8; ++t) {
        PACE(9 + n, t); A[t] = LDV(n + 1, t); FENCE();
        if (t == 6) { tg = __builtin_amdgcn_readfirstlane(cw_load(lds, CW_TG)); PV(B, n); }
      }
    }
#pragma unroll
    for (int t = 0; t < 8; ++t) {
      PACE(15, t); B[t] = LDV(NBLK - 1, t); FENCE();
      if (t == 6) PV(A, NBLK - 2);
    }
    if (lane == 0) cw_inc(lds, CW_NLAST);
    FENCE();
    const u32 t1 = cw_load(lds, CW_T1), t2 = cw_load(lds, CW_T2);
    int p1off = WS_P1 + ((g * 32 + (w & 3) * 8 + g4 * 2 + (lane & 1)) * 32 + j) * 128 + ((lane >> 1) & 7) * 16;
    LAS f32x4* rowp = (LAS f32x4*)(wreg + (lane & 3) * PROW + 32 * g4);
    const LAS f32x2* rdp = (const LAS f32x2*)&lds[L_W + (w & 3) * PROW + 2 * lane];
    const LAS f32x4* fp = (const LAS f32x4*)&lds[L_F + (w & 3) * NW];
    {
      v4u P = *(const v4u*)(wreg + POFF + (NBLK - 1) * 64 + g4 * 16 + (lane & 3) * 4);
      asm volatile("" : "+v"(P), "+v"(p1off), "+v"(rowp), "+v"(rdp), "+v"(fp));
      v4u fr[8];
#pragma unroll
      for (int u = 0; u < 3; ++u) {
#pragma unroll
        for (int e = 0; e < 4; ++e) { fr[2 * e][u] = lo16s(B[u][e], B[u + 4][e]); fr[2 * e + 1][u] = hi16s(B[u][e], B[u + 4][e]); }
        asm volatile("" : "+v"(fr[0][u]), "+v"(fr[1][u]), "+v"(fr[2][u]), "+v"(fr[3][u]), "+v"(fr[4][u]), "+v"(fr[5][u]), "+v"(fr[6][u]), "+v"(fr[7][u]));
        FENCE();
      }
#pragma unroll
      for (int e = 0; e < 4; ++e) { fr[2 * e][3] = lo16s(B[3][e], B[7][e]); fr[2 * e + 1][3] = hi16s(B[3][e], B[7][e]); }
#pragma unroll
      for (int i = 0; i < 8; ++i) acc[i] = mfma16(fr[i], P, acc[i]);
      FENCE();
#pragma unroll
      for (int i = 0; i < 8; ++i) rowp[i] = acc[i];
    }
    wg_barrier();
    if (w < 4) {
      f32x2 pv[NW];
#pragma unroll
      for (int i = 0; i < NW; ++i) pv[i] = rdp[i * (WREG / 2)];
      const f32x4 fa = fp[0], fb = fp[1];
      const f32x2 o = ((pv[0] * fa[0] + pv[1] * fa[1]) + (pv[2] * fa[2] + pv[3] * fa[3])) + ((pv[4] * fb[0] + pv[5] * fb[1]) + (pv[6] * fb[2] + pv[7] * fb[3]));
      st_sc1(rs, p1off, rec_make(__float_as_uint(o.x), __float_as_uint(o.y), t1, t2));
    }
    if (is_fin) {
      int off = WS_P2 + fin * 16384 + w * 2048 + lane * 16;
      asm volatile("" : "+v"(off));
      wg_barrier();
      __builtin_amdgcn_s_sleep(4);
      wait_until(tg, g_due(32) + G_TL + G_T2);
      const f32x2 s = gather_sum<2, 1024>(rs, off, t1, t2);
      if (lane < 4) *(f32x2*)&lds[L_FIN + w * 8 + lane * 2] = s;
      wg_barrier();
    }
  } else {
    __builtin_amdgcn_s_setprio(3);
    const u64 t0 = now();
    if (lane == 0) *(v4u*)&lds[L_CTL] = v4u{0u, 0u, 0u, (u32)t0};
    wg_barrier();
    FENCE();
    v4u wr[16], xr[2];
    {
      const __amdgpu_buffer_rsrc_t rq = __builtin_amdgcn_make_buffer_rsrc((void*)wq, 0, DM * HQ * DH * 2, 0x00020000);
      const int row0 = (j & 3) * 128 + (lane >> 3) * 16;
      const int vo = row0 * (HQ * DH * 2) + g * 1024 + (j >> 2) * 128 + (lane & 7) * 16;
#pragma unroll
      for (int r = 0; r < 16; ++r) wr[r] = __builtin_amdgcn_raw_buffer_load_b128(rq, vo, r * (HQ * DH * 2), AUX_NT);
      xr[0] = *(const GAS v4u*)(x + row0); xr[1] = *(const GAS v4u*)(x + row0 + 8);
    }
    const u64 ep = __hip_atomic_load((GAS u64*)(ws + WS_EPOCH), __ATOMIC_RELAXED, __HIP_MEMORY_SCOPE_AGENT);
    FENCE();
    float qv[8];
#pragma unroll
    for (int c = 0; c < 8; ++c) qv[c] = 0.f;
#pragma unroll
    for (int r = 0; r < 8; ++r)
#pragma unroll
      for (int e = 0; e < 4; ++e) {
        qv[2 * e] = dot2(lo16s(wr[2 * r][e], wr[2 * r + 1][e]), xr[r >> 2][r & 3], qv[2 * e]);
        qv[2 * e + 1] = dot2(hi16s(wr[2 * r][e], wr[2 * r + 1][e]), xr[r >> 2][r & 3], qv[2 * e + 1]);
      }
    float q4[4];
#pragma unroll
    for (int c = 0; c < 4; ++c) {
      const float a = qv[c] + dppf<ROR8>(qv[c]), b = qv[c + 4] + dppf<ROR8>(qv[c + 4]);
      q4[c] = xrow_sum((lane & 8) ? b : a);
    }
    u64 z = ep + 0x9E3779B97F4A7C15ull;
    z = (z ^ (z >> 30)) * 0xBF58476D1CE4E5B9ull; z = (z ^ (z >> 27)) * 0x94D049BB133111EBull; z ^= z >> 31;
    const u32 t1 = __builtin_amdgcn_readfirstlane((u32)z) | 1u, t2 = __builtin_amdgcn_readfirstlane((u32)(z >> 32));
    if (lane < 32)
      st_sc1(rs, WS_QX + ((g * 4 + (j & 3)) * 256 + (j >> 2) * 32 + (lane & 7) * 4 + ((lane >> 3) & 1) * 2 + (lane >> 4)) * 16,
             rec_make(__float_as_uint((lane & 16) ? q4[2] : q4[0]), __float_as_uint((lane & 16) ? q4[3] : q4[1]), t1, t2));
    if (lane == 0) { cw_store(lds, CW_T1, t1); cw_store(lds, CW_T2, t2); }
    {
      while (now() - t0 < T_Q) __builtin_amdgcn_s_sleep(2);
      const int off = WS_QX + (g * 1024 + lane) * 16;
      for (;;) {
        asm volatile("" ::: "memory");
#pragma unroll
        for (int i = 0; i < 16; ++i) wr[i] = __builtin_amdgcn_raw_buffer_load_b128(rs, off + (i & 3) * 1024, (i >> 2) * 4096, AUX_SC1);
        u32 bad = 0u;
#pragma unroll
        for (int i = 0; i < 16; ++i) bad |= rec_bad(wr[i], t1, t2);
        if (__all(bad == 0u)) break;
        if (now() - t0 > SPIN_CAP) break;
        __builtin_amdgcn_s_sleep(8);
      }
#pragma unroll
      for (int i = 0; i < 4; ++i) {
        const float a = (__uint_as_float(wr[i].x) + __uint_as_float(wr[4 + i].x)) + (__uint_as_float(wr[8 + i].x) + __uint_as_float(wr[12 + i].x));
        const float b = (__uint_as_float(wr[i].z) + __uint_as_float(wr[4 + i].z)) + (__uint_as_float(wr[8 + i].z) + __uint_as_float(wr[12 + i].z));
        *(u32*)&lds[L_QF + i * 64 + lane] = pack_bf16(a, b);
      }
    }
    asm volatile("s_waitcnt lgkmcnt(0)" ::: "memory");
    if (lane == 0) cw_store(lds, CW_QRDY, 1u);
    u32 tre;
    {
      cw_wait<8>(lds, CW_NML, NW);
      const f32x2 ml = *(const f32x2*)&lds[L_ML + ((lane >> 4) * NW + (lane & 7)) * 2];
      float M = ml.x;
      M = fmaxf(M, dppf<XOR1>(M)); M = fmaxf(M, dppf<XOR2>(M)); M = fmaxf(M, dppf<ROR4>(M));
      float L = ml.y * e2(ml.x - M);
      L += dppf<XOR1>(L); L += dppf<XOR2>(L); L += dppf<ROR4>(L);
      if ((lane & 15) == 0) st_sc1(rs, WS_ML + ((g * 5 + (lane >> 4)) * 32 + j) * 16, rec_make(__float_as_uint(M), __float_as_uint(L), t1, t2));
      const u64 tm = now();
      if (lane == 1) st_sc1(rs, WS_ML + ((g * 5 + 4) * 32 + j) * 16, rec_make((u32)tm, 0u, t1, t2));
      while (now() - tm < T_ML) __builtin_amdgcn_s_sleep(8);
      const int off = WS_ML + ((g * 5 + (lane >> 4)) * 32 + (lane & 15)) * 16;
      v4u r0, r1, r2;
      for (;;) {
        asm volatile("" ::: "memory");
        r2 = ld_sc1(rs, WS_ML + ((g * 5 + 4) * 32 + (lane & 31)) * 16);
        r0 = ld_sc1(rs, off); r1 = ld_sc1(rs, off + 256);
        if (__all((rec_bad(r0, t1, t2) | rec_bad(r1, t1, t2) | rec_bad(r2, t1, t2)) == 0u)) break;
        if (now() - tm > SPIN_CAP) break;
        __builtin_amdgcn_s_sleep(32);
      }
      float dt = (float)(int)(r2.x - (u32)tm);
      dt += dppf<XOR1>(dt); dt += dppf<XOR2>(dt); dt += dppf<ROR4>(dt); dt += dppf<ROR8>(dt); dt = xrow_sum(dt);
      tre = __builtin_amdgcn_readfirstlane((u32)tm + (u32)((int)(dt * (1.0f / 64.0f)) + G_C - g_due(18)));
      if (lane == 0) cw_store(lds, CW_TG, tre);
      float Mg = fmaxf(__uint_as_float(r0.x), __uint_as_float(r1.x));
      Mg = fmaxf(Mg, dppf<XOR1>(Mg)); Mg = fmaxf(Mg, dppf<XOR2>(Mg)); Mg = fmaxf(Mg, dppf<ROR4>(Mg)); Mg = fmaxf(Mg, dppf<ROR8>(Mg));
      float Lg = __uint_as_float(r0.z) * e2(__uint_as_float(r0.x) - Mg) + __uint_as_float(r1.z) * e2(__uint_as_float(r1.x) - Mg);
      Lg += dppf<XOR1>(Lg); Lg += dppf<XOR2>(Lg); Lg += dppf<ROR4>(Lg); Lg += dppf<ROR8>(Lg);
      if ((lane & 8) == 0) lds[L_F + (lane >> 4) * NW + (lane & 7)] = e2(ml.x - Mg) / Lg;
    }
    cw_wait<2>(lds, CW_NLAST, NW);
    {
      const __amdgpu_buffer_rsrc_t ro = __builtin_amdgcn_make_buffer_rsrc((void*)(wo + (size_t)(g * 512 + j * 16) * DM), 0, 16 * DM * 2, 0x00020000);
#pragma unroll
      for (int r = 0; r < 16; ++r) wr[r] = __builtin_amdgcn_raw_buffer_load_b128(ro, lane * 16 + (r & 3) * 1024, (r >> 2) * 4096, AUX_NT);
      FENCE();
    }
    wg_barrier();
    u32 wp[8][8];
#pragma unroll
    for (int r = 0; r < 8; ++r) {
#pragma unroll
      for (int e = 0; e < 4; ++e) { wp[r][2 * e] = lo16s(wr[r][e], wr[r + 8][e]); wp[r][2 * e + 1] = hi16s(wr[r][e], wr[r + 8][e]); }
      asm volatile("" : "+v"(wp[r][0]), "+v"(wp[r][1]), "+v"(wp[r][2]), "+v"(wp[r][3]), "+v"(wp[r][4]), "+v"(wp[r][5]), "+v"(wp[r][6]), "+v"(wp[r][7]));
    }
    FENCE();
    int offv = WS_P1 + ((g * 32 + j) * 32 + (lane >> 3) * 4) * 128 + (lane & 7) * 16;
    int p2off = WS_P2 + ((lane >> 2) * 256 + bid) * 64 + (lane & 3) * 16;
    asm volatile("" : "+v"(offv), "+v"(p2off));
    __builtin_amdgcn_s_sleep(10);
    wait_until(tre, g_due(32) + G_TL);
    const f32x2 o = gather_sum<4, 128>(rs, offv, t1, t2);
    const u32 opk = pack_bf16(o.x, o.y);
    u32 so[8];
#pragma unroll
    for (int r = 0; r < 8; ++r) so[r] = __builtin_amdgcn_readlane(opk, r);
    FENCE();
    float y[8];
#pragma unroll
    for (int c = 0; c < 8; ++c) y[c] = 0.f;
#pragma unroll
    for (int r = 0; r < 8; ++r)
#pragma unroll
      for (int c = 0; c < 8; ++c) y[c] = dot2(wp[r][c], so[r], y[c]);
#pragma unroll
    for (int i = 0; i < 4; ++i)
      __builtin_amdgcn_raw_buffer_store_b128(rec_make(__float_as_uint(y[2 * i]), __float_as_uint(y[2 * i + 1]), t1, t2), rs, p2off, i * (16 * 256 * 64), AUX_SC1);
    FENCE();
    if (is_fin) {
      wg_barrier();
      wg_barrier();
      if (lane < 4) {
        f32x2 v[NW];
#pragma unroll
        for (int i = 0; i < NW; ++i) v[i] = *(const f32x2*)&lds[L_FIN + i * 8 + lane * 2];
        const f32x2 s = ((v[0] + v[1]) + (v[2] + v[3])) + ((v[4] + v[5]) + (v[6] + v[7]));
        *(GAS u32*)(out + (fin & 15) * 32 + lane * 8 + (fin >> 4) * 2) = pack_bf16(s.x, s.y);
        if (bid == 0 && lane == 0) __hip_atomic_store((GAS u64*)(ws + WS_EPOCH), ep + 1ull, __ATOMIC_RELAXED, __HIP_MEMORY_SCOPE_AGENT);
      }
    }
  }
}

extern "C" void kernel_launch(void* const* d_in, const int* in_sizes, int n_in, void* d_out, int out_size, void* d_ws, size_t ws_size,
                              hipStream_t stream) {
  gqa_decode_kernel<<<NWG, NTHR, 0, stream>>>((const u16*)d_in[0], (const u16*)d_in[1], (const u16*)d_in[2], (const u16*)d_in[3],
                                              (const u16*)d_in[4], (u16*)d_out, (unsigned char*)d_ws);
}
```

```cpp
#define LK 0
#include <hip/hip_runtime.h>
#include <hip/hip_bf16.h>
#include <stdint.h>

typedef unsigned short u16;
typedef unsigned int u32;
typedef unsigned long long u64;
typedef u32 v4u __attribute__((ext_vector_type(4)));
typedef float f32x4 __attribute__((ext_vector_type(4)));
typedef float f32x2 __attribute__((ext_vector_type(2)));
typedef __bf16 bf16x8 __attribute__((ext_vector_type(8)));
typedef __bf16 bf16x2 __attribute__((ext_vector_type(2)));
#define GAS __attribute__((address_space(1)))
#define LAS __attribute__((address_space(3)))

constexpr int DM = 512, HQ = 32, SK = 65536, DH = 128;
constexpr int NWG = 256, NW = 8, NTHR = (NW + 1) * 64;
constexpr int NBLK = SK / 32 / 32 / NW;
constexpr float C2 = 0.088388347648318440f * 1.4426950408889634f;

#define KV_PAT "axwwwyypxqrxcb"
constexpr int pat_stride(char ch, int idx) {
  for (int i = 0; i < 14; ++i) if (KV_PAT[i] == ch && idx-- == 0) return 1024 << i;
  return 0;
}
constexpr int toff(int t) { return (t & 1) * pat_stride('a', 0) + ((t >> 1) & 1) * pat_stride('b', 0) + (t >> 2) * pat_stride('c', 0); }
__device__ __forceinline__ int noff(int n) { return (n & 1) * pat_stride('p', 0) + ((n >> 1) & 1) * pat_stride('q', 0) + (n >> 2) * pat_stride('r', 0); }

constexpr int G_S0 = 145, G_PER = 215;
constexpr int G_D2 = 90;
constexpr int G_C = 75;
constexpr int G_TL = 117, G_T2 = 92;
constexpr int T_Q = 600, T_ML = 300;
constexpr int g_due(int hb) { return G_S0 + ((hb * G_PER) >> 1); }
constexpr u64 SPIN_CAP = 20000000ull;

constexpr int WS_EPOCH = 0;
constexpr int WS_ML = 4096;
constexpr int WS_QX = 32768;
constexpr int WS_P1 = 262144;
constexpr int WS_P2 = WS_P1 + (1 << 20);
constexpr int WS_END = WS_P2 + (1 << 20);

constexpr int PROW = 132, POFF = 544;
constexpr int WREG = POFF + 512;
constexpr int L_W = 0;
constexpr int L_ML = L_W + NW * WREG;
constexpr int L_F = L_ML + NW * 8;
constexpr int L_QF = L_F + NW * 4;
constexpr int L_CTL = L_QF + 256;
constexpr int L_FIN = L_CTL + 8;
constexpr int L_END = L_FIN + NW * 8;
constexpr int CW_NML = 0, CW_NLAST = 1, CW_QRDY = 2, CW_TG = 3, CW_T1 = 4, CW_T2 = 5;

__device__ __forceinline__ f32x4 mfma16(v4u a, v4u b, f32x4 c) {
  return __builtin_amdgcn_mfma_f32_16x16x32_bf16(__builtin_bit_cast(bf16x8, a), __builtin_bit_cast(bf16x8, b), c, 0, 0, 0);
}
__device__ __forceinline__ u32 pack_bf16(float lo, float hi) {
  bf16x2 r; r.x = (__bf16)lo; r.y = (__bf16)hi; return __builtin_bit_cast(u32, r);
}
__device__ __forceinline__ float dot2(u32 a, u32 b, float c) {
  return __builtin_amdgcn_fdot2_f32_bf16(__builtin_bit_cast(bf16x2, a), __builtin_bit_cast(bf16x2, b), c, false);
}
__device__ __forceinline__ u32 lo16s(u32 a, u32 b) { return __builtin_amdgcn_perm(b, a, 0x05040100u); }
__device__ __forceinline__ u32 hi16s(u32 a, u32 b) { return __builtin_amdgcn_perm(b, a, 0x07060302u); }
template <int CTRL> __device__ __forceinline__ float dppf(float x) {
  return __builtin_bit_cast(float, __builtin_amdgcn_mov_dpp(__builtin_bit_cast(int, x), CTRL, 0xf, 0xf, true));
}
template <int BANK> __device__ __forceinline__ float bank_put(float old, float src) {
  return __builtin_bit_cast(float, __builtin_amdgcn_update_dpp(__builtin_bit_cast(int, old), __builtin_bit_cast(int, src), 0xE4, 0xf, 1 << BANK, false));
}
constexpr int ROR4 = 0x124, ROR8 = 0x128, ROR12 = 0x12C, XOR1 = 0xB1, XOR2 = 0x4E;
__device__ __forceinline__ float xrow_max(float x) {
  auto s = __builtin_amdgcn_permlane16_swap(__float_as_uint(x), __float_as_uint(x), false, false);
  x = fmaxf(__uint_as_float(s[0]), __uint_as_float(s[1]));
  auto t = __builtin_amdgcn_permlane32_swap(__float_as_uint(x), __float_as_uint(x), false, false);
  return fmaxf(__uint_as_float(t[0]), __uint_as_float(t[1]));
}
__device__ __forceinline__ float xrow_sum(float x) {
  auto s = __builtin_amdgcn_permlane16_swap(__float_as_uint(x), __float_as_uint(x), false, false);
  x = __uint_as_float(s[0]) + __uint_as_float(s[1]);
  auto t = __builtin_amdgcn_permlane32_swap(__float_as_uint(x), __float_as_uint(x), false, false);
  return __uint_as_float(t[0]) + __uint_as_float(t[1]);
}
__device__ __forceinline__ float e2(float x) { return __builtin_amdgcn_exp2f(x); }
__device__ __forceinline__ u64 now() { return __builtin_amdgcn_s_memrealtime(); }
__device__ __forceinline__ void wg_barrier() {
  asm volatile("s_waitcnt lgkmcnt(0)" ::: "memory");
  __builtin_amdgcn_s_barrier();
  asm volatile("" ::: "memory");
}
#define FENCE() __builtin_amdgcn_sched_barrier(0)
__device__ __forceinline__ u32 cw_load(float* lds, int i) { return __hip_atomic_load((u32*)&lds[L_CTL + i], __ATOMIC_RELAXED, __HIP_MEMORY_SCOPE_WORKGROUP); }
__device__ __forceinline__ void cw_store(float* lds, int i, u32 v) { __hip_atomic_store((u32*)&lds[L_CTL + i], v, __ATOMIC_RELAXED, __HIP_MEMORY_SCOPE_WORKGROUP); }
__device__ __forceinline__ void cw_inc(float* lds, int i) { __hip_atomic_fetch_add((u32*)&lds[L_CTL + i], 1u, __ATOMIC_RELAXED, __HIP_MEMORY_SCOPE_WORKGROUP); }
template <int SLEEP> __device__ __forceinline__ void cw_wait(float* lds, int i, u32 want) {
  const u64 tb = now();
  while (cw_load(lds, i) < want) {
    if (now() - tb > 2 * SPIN_CAP) break;
    __builtin_amdgcn_s_sleep(SLEEP);
  }
  asm volatile("" ::: "memory");
}

__device__ __forceinline__ v4u rec_make(u32 a, u32 b, u32 t1, u32 t2) { return v4u{a, t1 ^ b, b, t2 ^ a}; }
__device__ __forceinline__ u32 rec_bad(v4u r, u32 t1, u32 t2) { return (r.y ^ r.z ^ t1) | (r.w ^ r.x ^ t2); }
constexpr int AUX_NT = 2, AUX_SC1 = 16;
__device__ __forceinline__ v4u ld_sc1(__amdgpu_buffer_rsrc_t rs, int off) { return __builtin_amdgcn_raw_buffer_load_b128(rs, off, 0, AUX_SC1); }
__device__ __forceinline__ void st_sc1(__amdgpu_buffer_rsrc_t rs, int off, v4u v) { __builtin_amdgcn_raw_buffer_store_b128(v, rs, off, 0, AUX_SC1); }
template <int N, int STRIDE> __device__ __forceinline__ f32x2 gather_sum(__amdgpu_buffer_rsrc_t rs, int off, u32 t1, u32 t2) {
  static_assert(N == 2 || N == 4, "");
  const u64 tb = now();
  v4u r[N];
  bool ok = false;
  for (;;) {
    asm volatile("" ::: "memory");
    if (!ok) {
#pragma unroll
      for (int i = 0; i < N; ++i) r[i] = ld_sc1(rs, off + i * STRIDE);
      u32 bad = 0u;
#pragma unroll
      for (int i = 0; i < N; ++i) bad |= rec_bad(r[i], t1, t2);
      ok = bad == 0u;
    }
    if (__all(ok)) break;
    if (now() - tb > SPIN_CAP) break;
  }
  float a = __uint_as_float(r[0].x) + __uint_as_float(r[1].x), b = __uint_as_float(r[0].z) + __uint_as_float(r[1].z);
  if constexpr (N == 4) { a += __uint_as_float(r[2].x) + __uint_as_float(r[3].x); b += __uint_as_float(r[2].z) + __uint_as_float(r[3].z); }
  else { a += dppf<ROR4>(a); b += dppf<ROR4>(b); }
  a += dppf<ROR8>(a); b += dppf<ROR8>(b);
  return f32x2{xrow_sum(a), xrow_sum(b)};
}
__device__ __forceinline__ void wait_until(u32 base, int ticks) {
  for (int i = 0; i < 256 && (int)((u32)now() - base) < ticks; ++i) __builtin_amdgcn_s_sleep(1);
}

__global__ void __launch_bounds__(NTHR, 3) gqa_decode_kernel(const u16* __restrict__ x, const u16* __restrict__ kc, const u16* __restrict__ vc,
                                                             const u16* __restrict__ wq, const u16* __restrict__ wo, u16* __restrict__ out,
                                                             unsigned char* ws) {
  __shared__ __attribute__((aligned(16))) float lds[L_END];
  u64 tA, tB;
  asm volatile("s_memrealtime %0\n\ts_waitcnt lgkmcnt(0)\n\ts_memrealtime %1\n\ts_waitcnt lgkmcnt(0)" : "=&s"(tA), "=&s"(tB));
  __builtin_amdgcn_sched_barrier(0);
  const int tid = threadIdx.x, lane = tid & 63, wv = __builtin_amdgcn_readfirstlane(tid >> 6);
  const int bid = blockIdx.x, g = bid >> 5, j = bid & 31;
  const int w = wv - 1, g4 = lane >> 4;
  int kl = ((lane >> 2) & 3) * 256 + g4 * 64 + (lane & 3) * 16;
  int sb = g * (SK * DH * 2) + (j & 1) * pat_stride('x', 0) + ((j >> 1) & 1) * pat_stride('x', 1) + ((j >> 2) & 1) * pat_stride('x', 2) + ((j >> 3) & 1) * pat_stride('y', 0) +
           (j >> 4) * pat_stride('y', 1) + (w & 1) * pat_stride('w', 0) + ((w >> 1) & 1) * pat_stride('w', 1) + ((w >> 2) & 1) * pat_stride('w', 2);
  asm volatile("" : "+v"(kl));
  asm volatile("" : "+s"(sb));
  asm volatile("" :: "s"(x), "s"(kc), "s"(vc), "s"(wq), "s"(wo), "s"(out), "s"(ws));
  const bool is_fin = (bid & 3) == ((bid >> 3) & 3);
  const int fin = bid >> 2;
  const __amdgpu_buffer_rsrc_t rs = __builtin_amdgcn_make_buffer_rsrc((void*)ws, 0, WS_END, 0x00020000);

  if (wv > 0) {
    const __amdgpu_buffer_rsrc_t rk = __builtin_amdgcn_make_buffer_rsrc((void*)kc, 0, 8 * SK * DH * 2, 0x00020000);
    const __amdgpu_buffer_rsrc_t rv = __builtin_amdgcn_make_buffer_rsrc((void*)vc, 0, 8 * SK * DH * 2, 0x00020000);
    const int vl = lane * 16;
#define LDK(n, t) __builtin_amdgcn_raw_buffer_load_b128(rk, kl, sb + noff(n) + toff(t), AUX_NT)
#define LDV(n, t) __builtin_amdgcn_raw_buffer_load_b128(rv, vl, sb + noff(n) + toff(t), AUX_NT)
    u32 tg = (u32)now();
    int lead = 0;
    const int gd2 = G_D2;
#define PACE(blk, t) do {                                                                                                                  \
      if (((t) & 3) == 0) { lead = g_due(2 * (blk) + ((t) >> 2)) - (int)((u32)now() - tg); FENCE(); }                                      \
      if ((blk) >= 3)                                                                                                                      \
        asm volatile("s_waitcnt vmcnt(3)\n\ts_cmp_gt_i32 %0, 0\n\ts_cbranch_scc0 1f\n\ts_waitcnt vmcnt(2)\n\ts_cmp_gt_i32 %0, %1\n\t"      \
                     "s_cbranch_scc0 1f\n\ts_waitcnt vmcnt(1)\n1:" :: "s"(lead), "s"(gd2) : "memory", "scc");                              \
      else                                                                                                                                 \
        asm volatile("s_waitcnt vmcnt(3)\n\ts_cmp_gt_i32 %0, 0\n\ts_cbranch_scc0 1f\n\ts_waitcnt vmcnt(2)\n1:" :: "s"(lead) : "memory", "scc"); \
      FENCE(); } while (0)
    v4u A[8], B[8], C[8], D[8];
    float* const wreg = &lds[L_W + w * WREG];
#pragma unroll
    for (int t = 0; t < 8; ++t) {
      if (t >= 4) { asm volatile("s_waitcnt vmcnt(3)" ::: "memory"); FENCE(); }
      A[t] = LDK(0, t); FENCE();
      if (t == 3) { wg_barrier(); FENCE(); }
    }
#pragma unroll
    for (int t = 0; t < 8; ++t) { PACE(1, t); B[t] = LDK(1, t); FENCE(); }
#pragma unroll
    for (int t = 0; t < 8; ++t) { PACE(2, t); C[t] = LDK(2, t); FENCE(); }
#pragma unroll
    for (int t = 0; t < 8; ++t) { PACE(3, t); D[t] = LDK(3, t); FENCE(); }
    cw_wait<1>(lds, CW_QRDY, 1u);
    const v4u qf = *(const v4u*)&lds[L_QF + (lane & 3) * 64 + g4 * 16 + ((lane >> 2) & 3) * 4];
    float sc[NBLK][2];

#define QKH(KK, n, G) do {                                                                                  \
      f32x4 d[4];                                                                                           \
      _Pragma("unroll") for (int t = 0; t < 4; ++t) d[t] = mfma16(KK[4 * (G) + t], qf, f32x4{0.f, 0.f, 0.f, 0.f}); \
      float T[4];                                                                                           \
      _Pragma("unroll") for (int r = 0; r < 4; ++r) {                                                       \
        float tt = d[r][0];                                                                                 \
        tt = bank_put<1>(tt, d[(1 + r) & 3][1]);                                                            \
        tt = bank_put<2>(tt, d[(2 + r) & 3][2]);                                                            \
        tt = bank_put<3>(tt, d[(3 + r) & 3][3]);                                                            \
        T[r] = tt; }                                                                                        \
      sc[n][G] = (T[0] + dppf<ROR4>(T[1])) + (dppf<ROR8>(T[2]) + dppf<ROR12>(T[3]));                        \
      asm volatile("" : "+v"(sc[n][G]));                                                                    \
      FENCE();                                                                                              \
    } while (0)
#define PV(VV, n) do {                                                                                      \
      const v4u P = *(const v4u*)(wreg + POFF + (n) * 64 + g4 * 16 + (lane & 3) * 4);                       \
      _Pragma("unroll") for (int e = 0; e < 4; ++e) {                                                       \
        const v4u lo = {lo16s(VV[0][e], VV[4][e]), lo16s(VV[1][e], VV[5][e]), lo16s(VV[2][e], VV[6][e]), lo16s(VV[3][e], VV[7][e])}; \
        const v4u hi = {hi16s(VV[0][e], VV[4][e]), hi16s(VV[1][e], VV[5][e]), hi16s(VV[2][e], VV[6][e]), hi16s(VV[3][e], VV[7][e])}; \
        acc[2 * e] = mfma16(lo, P, acc[2 * e]);                                                             \
        acc[2 * e + 1] = mfma16(hi, P, acc[2 * e + 1]); }                                                   \
      FENCE();                                                                                              \
    } while (0)
#define STEP(X, nx, Y, ny) _Pragma("unroll") for (int t = 0; t < 8; ++t) { PACE(nx, t); X[t] = LDK(nx, t); FENCE(); if (t == 2) QKH(Y, ny, 0); if (t == 6) QKH(Y, ny, 1); }

    QKH(A, 0, 0);
#pragma unroll
    for (int t = 0; t < 8; ++t) {
      PACE(4, t); A[t] = LDK(4, t); FENCE();
      if (t == 0) QKH(A, 0, 1);
      if (t == 1) QKH(B, 1, 0);
      if (t == 2) QKH(B, 1, 1);
      if (t == 3) QKH(C, 2, 0);
      if (t == 4) QKH(C, 2, 1);
      if (t == 5) QKH(D, 3, 0);
      if (t == 6) QKH(D, 3, 1);
    }
    STEP(B, 5, A, 4)
    STEP(A, 6, B, 5)
    STEP(B, 7, A, 6)
#pragma unroll
    for (int t = 0; t < 8; ++t) {
      PACE(8, t); A[t] = LDV(0, t); FENCE();
      if (t == 2) QKH(B, 7, 0);
      if (t == 4) QKH(B, 7, 1);
      if (t == 5) {
        float mx = fmaxf(sc[0][0], sc[0][1]);
#pragma unroll
        for (int n = 1; n < NBLK; ++n) mx = fmaxf(mx, fmaxf(sc[n][0], sc[n][1]));
        mx = fmaxf(mx, dppf<ROR4>(mx)); mx = fmaxf(mx, dppf<ROR8>(mx)); mx = xrow_max(mx);
        const float m = mx * C2;
        float l = 0.f;
#pragma unroll
        for (int n = 0; n < NBLK; ++n) {
          const float p0 = e2(fmaf(sc[n][0], C2, -m)), p1 = e2(fmaf(sc[n][1], C2, -m));
          l += p0 + p1;
          *(u32*)(wreg + POFF + n * 64 + g4 * 16 + (lane & 3) * 4 + ((lane >> 2) & 3)) = pack_bf16(p0, p1);
        }
        l += dppf<ROR4>(l); l += dppf<ROR8>(l); l = xrow_sum(l);
        if (lane < 4) *(f32x2*)&lds[L_ML + (lane * NW + w) * 2] = f32x2{m, l};
        asm volatile("s_waitcnt lgkmcnt(0)" ::: "memory");
        if (lane == 0) cw_inc(lds, CW_NML);
        FENCE();
      }
    }
    f32x4 acc[8];
#pragma unroll
    for (int i = 0; i < 8; ++i) acc[i] = f32x4{0.f, 0.f, 0.f, 0.f};
#pragma unroll 1
    for (int n = 1; n < NBLK - 1; n += 2) {
#pragma unroll
      for (int t = 0; t < 8; ++t) {
        PACE(8 + n, t); B[t] = LDV(n, t); FENCE();
        if (t == 6) { tg = __builtin_amdgcn_readfirstlane(cw_load(lds, CW_TG)); PV(A, n - 1); }
      }
#pragma unroll
      for (int t = 0; t < 8; ++t) {
        PACE(9 + n, t); A[t] = LDV(n + 1, t); FENCE();
        if (t == 6) { tg = __builtin_amdgcn_readfirstlane(cw_load(lds, CW_TG)); PV(B, n); }
      }
    }
#pragma unroll
    for (int t = 0; t < 8; ++t) {
      PACE(15, t); B[t] = LDV(NBLK - 1, t); FENCE();
      if (t == 6) PV(A, NBLK - 2);
    }
    if (lane == 0) cw_inc(lds, CW_NLAST);
    FENCE();
    const u32 t1 = cw_load(lds, CW_T1), t2 = cw_load(lds, CW_T2);
    int p1off = WS_P1 + ((g * 32 + (w & 3) * 8 + g4 * 2 + (lane & 1)) * 32 + j) * 128 + ((lane >> 1) & 7) * 16;
    LAS f32x4* rowp = (LAS f32x4*)(wreg + (lane & 3) * PROW + 32 * g4);
    const LAS f32x2* rdp = (const LAS f32x2*)&lds[L_W + (w & 3) * PROW + 2 * lane];
    const LAS f32x4* fp = (const LAS f32x4*)&lds[L_F + (w & 3) * NW];
    {
      v4u P = *(const v4u*)(wreg + POFF + (NBLK - 1) * 64 + g4 * 16 + (lane & 3) * 4);
      asm volatile("" : "+v"(P), "+v"(p1off), "+v"(rowp), "+v"(rdp), "+v"(fp));
      v4u fr[8];
#pragma unroll
      for (int u = 0; u < 3; ++u) {
#pragma unroll
        for (int e = 0; e < 4; ++e) { fr[2 * e][u] = lo16s(B[u][e], B[u + 4][e]); fr[2 * e + 1][u] = hi16s(B[u][e], B[u + 4][e]); }
        asm volatile("" : "+v"(fr[0][u]), "+v"(fr[1][u]), "+v"(fr[2][u]), "+v"(fr[3][u]), "+v"(fr[4][u]), "+v"(fr[5][u]), "+v"(fr[6][u]), "+v"(fr[7][u]));
        FENCE();
      }
#pragma unroll
      for (int e = 0; e < 4; ++e) { fr[2 * e][3] = lo16s(B[3][e], B[7][e]); fr[2 * e + 1][3] = hi16s(B[3][e], B[7][e]); }
#pragma unroll
      for (int i = 0; i < 8; ++i) acc[i] = mfma16(fr[i], P, acc[i]);
      FENCE();
#pragma unroll
      for (int i = 0; i < 8; ++i) rowp[i] = acc[i];
    }
    wg_barrier();
    if (w < 4) {
      f32x2 pv[NW];
#pragma unroll
      for (int i = 0; i < NW; ++i) pv[i] = rdp[i * (WREG / 2)];
      const f32x4 fa = fp[0], fb = fp[1];
      const f32x2 o = ((pv[0] * fa[0] + pv[1] * fa[1]) + (pv[2] * fa[2] + pv[3] * fa[3])) + ((pv[4] * fb[0] + pv[5] * fb[1]) + (pv[6] * fb[2] + pv[7] * fb[3]));
      st_sc1(rs, p1off, rec_make(__float_as_uint(o.x), __float_as_uint(o.y), t1, t2));
    }
    if (is_fin) {
      int off = WS_P2 + fin * 16384 + w * 2048 + lane * 16;
      asm volatile("" : "+v"(off));
      wg_barrier();
      __builtin_amdgcn_s_sleep(4);
      wait_until(tg, g_due(32) + G_TL + G_T2);
      const f32x2 s = gather_sum<2, 1024>(rs, off, t1, t2);
      if (lane < 4) *(f32x2*)&lds[L_FIN + w * 8 + lane * 2] = s;
      wg_barrier();
    }
  } else {
    __builtin_amdgcn_s_setprio(3);
    const u64 t0 = now();
    if (lane == 0) *(v4u*)&lds[L_CTL] = v4u{0u, 0u, 0u, (u32)t0};
    wg_barrier();
    FENCE();
    v4u wr[16], xr[2];
    {
      const __amdgpu_buffer_rsrc_t rq = __builtin_amdgcn_make_buffer_rsrc((void*)wq, 0, DM * HQ * DH * 2, 0x00020000);
      const int row0 = (j & 3) * 128 + (lane >> 3) * 16;
      const int vo = row0 * (HQ * DH * 2) + g * 1024 + (j >> 2) * 128 + (lane & 7) * 16;
#pragma unroll
      for (int r = 0; r < 16; ++r) wr[r] = __builtin_amdgcn_raw_buffer_load_b128(rq, vo, r * (HQ * DH * 2), AUX_NT);
      xr[0] = *(const GAS v4u*)(x + row0); xr[1] = *(const GAS v4u*)(x + row0 + 8);
    }
    const u64 ep = __hip_atomic_load((GAS u64*)(ws + WS_EPOCH), __ATOMIC_RELAXED, __HIP_MEMORY_SCOPE_AGENT);
    FENCE();
    float qv[8];
#pragma unroll
    for (int c = 0; c < 8; ++c) qv[c] = 0.f;
#pragma unroll
    for (int r = 0; r < 8; ++r)
#pragma unroll
      for (int e = 0; e < 4; ++e) {
        qv[2 * e] = dot2(lo16s(wr[2 * r][e], wr[2 * r + 1][e]), xr[r >> 2][r & 3], qv[2 * e]);
        qv[2 * e + 1] = dot2(hi16s(wr[2 * r][e], wr[2 * r + 1][e]), xr[r >> 2][r & 3], qv[2 * e + 1]);
      }
    float q4[4];
#pragma unroll
    for (int c = 0; c < 4; ++c) {
      const float a = qv[c] + dppf<ROR8>(qv[c]), b = qv[c + 4] + dppf<ROR8>(qv[c + 4]);
      q4[c] = xrow_sum((lane & 8) ? b : a);
    }
    u64 z = ep + 0x9E3779B97F4A7C15ull;
    z = (z ^ (z >> 30)) * 0xBF58476D1CE4E5B9ull; z = (z ^ (z >> 27)) * 0x94D049BB133111EBull; z ^= z >> 31;
    const u32 t1 = __builtin_amdgcn_readfirstlane((u32)z) | 1u, t2 = __builtin_amdgcn_readfirstlane((u32)(z >> 32));
    if (lane < 32)
      st_sc1(rs, WS_QX + ((g * 4 + (j & 3)) * 256 + (j >> 2) * 32 + (lane & 7) * 4 + ((lane >> 3) & 1) * 2 + (lane >> 4)) * 16,
             rec_make(__float_as_uint((lane & 16) ? q4[2] : q4[0]), __float_as_uint((lane & 16) ? q4[3] : q4[1]), t1, t2));
    if (lane == 0) { cw_store(lds, CW_T1, t1); cw_store(lds, CW_T2, t2); }
    {
      while (now() - t0 < T_Q) __builtin_amdgcn_s_sleep(2);
      const int off = WS_QX + (g * 1024 + lane) * 16;
      for (;;) {
        asm volatile("" ::: "memory");
#pragma unroll
        for (int i = 0; i < 16; ++i) wr[i] = __builtin_amdgcn_raw_buffer_load_b128(rs, off + (i & 3) * 1024, (i >> 2) * 4096, AUX_SC1);
        u32 bad = 0u;
#pragma unroll
        for (int i = 0; i < 16; ++i) bad |= rec_bad(wr[i], t1, t2);
        if (__all(bad == 0u)) break;
        if (now() - t0 > SPIN_CAP) break;
        __builtin_amdgcn_s_sleep(8);
      }
#pragma unroll
      for (int i = 0; i < 4; ++i) {
        const float a = (__uint_as_float(wr[i].x) + __uint_as_float(wr[4 + i].x)) + (__uint_as_float(wr[8 + i].x) + __uint_as_float(wr[12 + i].x));
        const float b = (__uint_as_float(wr[i].z) + __uint_as_float(wr[4 + i].z)) + (__uint_as_float(wr[8 + i].z) + __uint_as_float(wr[12 + i].z));
        *(u32*)&lds[L_QF + i * 64 + lane] = pack_bf16(a, b);
      }
    }
    asm volatile("s_waitcnt lgkmcnt(0)" ::: "memory");
    if (lane == 0) cw_store(lds, CW_QRDY, 1u);
    u32 tre;
    {
      cw_wait<8>(lds, CW_NML, NW);
      const f32x2 ml = *(const f32x2*)&lds[L_ML + ((lane >> 4) * NW + (lane & 7)) * 2];
      float M = ml.x;
      M = fmaxf(M, dppf<XOR1>(M)); M = fmaxf(M, dppf<XOR2>(M)); M = fmaxf(M, dppf<ROR4>(M));
      float L = ml.y * e2(ml.x - M);
      L += dppf<XOR1>(L); L += dppf<XOR2>(L); L += dppf<ROR4>(L);
      if ((lane & 15) == 0) st_sc1(rs, WS_ML + ((g * 5 + (lane >> 4)) * 32 + j) * 16, rec_make(__float_as_uint(M), __float_as_uint(L), t1, t2));
      const u64 tm = now();
      if (lane == 1) st_sc1(rs, WS_ML + ((g * 5 + 4) * 32 + j) * 16, rec_make((u32)tm, 0u, t1, t2));
      while (now() - tm < T_ML) __builtin_amdgcn_s_sleep(8);
      const int off = WS_ML + ((g * 5 + (lane >> 4)) * 32 + (lane & 15)) * 16;
      v4u r0, r1, r2;
      for (;;) {
        asm volatile("" ::: "memory");
        r2 = ld_sc1(rs, WS_ML + ((g * 5 + 4) * 32 + (lane & 31)) * 16);
        r0 = ld_sc1(rs, off); r1 = ld_sc1(rs, off + 256);
        if (__all((rec_bad(r0, t1, t2) | rec_bad(r1, t1, t2) | rec_bad(r2, t1, t2)) == 0u)) break;
        if (now() - tm > SPIN_CAP) break;
        __builtin_amdgcn_s_sleep(32);
      }
      float dt = (float)(int)(r2.x - (u32)tm);
      dt += dppf<XOR1>(dt); dt += dppf<XOR2>(dt); dt += dppf<ROR4>(dt); dt += dppf<ROR8>(dt); dt = xrow_sum(dt);
      tre = __builtin_amdgcn_readfirstlane((u32)tm + (u32)((int)(dt * (1.0f / 64.0f)) + G_C - g_due(18)));
      if (lane == 0) cw_store(lds, CW_TG, tre);
      float Mg = fmaxf(__uint_as_float(r0.x), __uint_as_float(r1.x));
      Mg = fmaxf(Mg, dppf<XOR1>(Mg)); Mg = fmaxf(Mg, dppf<XOR2>(Mg)); Mg = fmaxf(Mg, dppf<ROR4>(Mg)); Mg = fmaxf(Mg, dppf<ROR8>(Mg));
      float Lg = __uint_as_float(r0.z) * e2(__uint_as_float(r0.x) - Mg) + __uint_as_float(r1.z) * e2(__uint_as_float(r1.x) - Mg);
      Lg += dppf<XOR1>(Lg); Lg += dppf<XOR2>(Lg); Lg += dppf<ROR4>(Lg); Lg += dppf<ROR8>(Lg);
      if ((lane & 8) == 0) lds[L_F + (lane >> 4) * NW + (lane & 7)] = e2(ml.x - Mg) / Lg;
    }
    cw_wait<2>(lds, CW_NLAST, NW);
    {
      const __amdgpu_buffer_rsrc_t ro = __builtin_amdgcn_make_buffer_rsrc((void*)(wo + (size_t)(g * 512 + j * 16) * DM), 0, 16 * DM * 2, 0x00020000);
#pragma unroll
      for (int r = 0; r < 16; ++r) wr[r] = __builtin_amdgcn_raw_buffer_load_b128(ro, lane * 16 + (r & 3) * 1024, (r >> 2) * 4096, AUX_NT);
      FENCE();
    }
    wg_barrier();
    u32 wp[8][8];
#pragma unroll
    for (int r = 0; r < 8; ++r) {
#pragma unroll
      for (int e = 0; e < 4; ++e) { wp[r][2 * e] = lo16s(wr[r][e], wr[r + 8][e]); wp[r][2 * e + 1] = hi16s(wr[r][e], wr[r + 8][e]); }
      asm volatile("" : "+v"(wp[r][0]), "+v"(wp[r][1]), "+v"(wp[r][2]), "+v"(wp[r][3]), "+v"(wp[r][4]), "+v"(wp[r][5]), "+v"(wp[r][6]), "+v"(wp[r][7]));
    }
    FENCE();
    int offv = WS_P1 + ((g * 32 + j) * 32 + (lane >> 3) * 4) * 128 + (lane & 7) * 16;
    int p2off = WS_P2 + ((lane >> 2) * 256 + bid) * 64 + (lane & 3) * 16;
    asm volatile("" : "+v"(offv), "+v"(p2off));
    __builtin_amdgcn_s_sleep(10);
    wait_until(tre, g_due(32) + G_TL);
    const f32x2 o = gather_sum<4, 128>(rs, offv, t1, t2);
    const u32 opk = pack_bf16(o.x, o.y);
    u32 so[8];
#pragma unroll
    for (int r = 0; r < 8; ++r) so[r] = __builtin_amdgcn_readlane(opk, r);
    FENCE();
    float y[8];
#pragma unroll
    for (int c = 0; c < 8; ++c) y[c] = 0.f;
#pragma unroll
    for (int r = 0; r < 8; ++r)
#pragma unroll
      for (int c = 0; c < 8; ++c) y[c] = dot2(wp[r][c], so[r], y[c]);
#pragma unroll
    for (int i = 0; i < 4; ++i)
      __builtin_amdgcn_raw_buffer_store_b128(rec_make(__float_as_uint(y[2 * i]), __float_as_uint(y[2 * i + 1]), t1, t2), rs, p2off, i * (16 * 256 * 64), AUX_SC1);
    FENCE();
    if (is_fin) {
      wg_barrier();
      wg_barrier();
      if (lane < 4) {
        f32x2 v[NW];
#pragma unroll
        for (int i = 0; i < NW; ++i) v[i] = *(const f32x2*)&lds[L_FIN + i * 8 + lane * 2];
        const f32x2 s = ((v[0] + v[1]) + (v[2] + v[3])) + ((v[4] + v[5]) + (v[6] + v[7]));
        *(GAS u32*)(out + (fin & 15) * 32 + lane * 8 + (fin >> 4) * 2) = pack_bf16(s.x, s.y);
        if (bid == 0 && lane == 0) __hip_atomic_store((GAS u64*)(ws + WS_EPOCH), ep + 1ull, __ATOMIC_RELAXED, __HIP_MEMORY_SCOPE_AGENT);
      }
    }
    if (bid == 0) { const u64 te = now(); while (now() - te < 10ull * (LK == 0 ? tB - tA : t0 - tB)) __builtin_amdgcn_s_sleep(1); }
  }
}

extern "C" void kernel_launch(void* const* d_in, const int* in_sizes, int n_in, void* d_out, int out_size, void* d_ws, size_t ws_size,
                              hipStream_t stream) {
  gqa_decode_kernel<<<NWG, NTHR, 0, stream>>>((const u16*)d_in[0], (const u16*)d_in[1], (const u16*)d_in[2], (const u16*)d_in[3],
                                              (const u16*)d_in[4], (u16*)d_out, (unsigned char*)d_ws);
}
```

```cpp
#include <hip/hip_runtime.h>
#include <hip/hip_bf16.h>
#include <stdint.h>

typedef unsigned short u16;
typedef unsigned int u32;
typedef unsigned long long u64;
typedef u32 v4u __attribute__((ext_vector_type(4)));
typedef float f32x4 __attribute__((ext_vector_type(4)));
typedef float f32x2 __attribute__((ext_vector_type(2)));
typedef __bf16 bf16x8 __attribute__((ext_vector_type(8)));
typedef __bf16 bf16x2 __attribute__((ext_vector_type(2)));
#define GAS __attribute__((address_space(1)))
#define LAS __attribute__((address_space(3)))

constexpr int DM = 512, HQ = 32, SK = 65536, DH = 128;
constexpr int NWG = 256, NW = 8, NTHR = (NW + 1) * 64;
constexpr int NBLK = SK / 32 / 32 / NW;
constexpr float C2 = 0.088388347648318440f * 1.4426950408889634f;

#define KV_PAT "axwwwyypxqrxcb"
constexpr int pat_stride(char ch, int idx) {
  for (int i = 0; i < 14; ++i) if (KV_PAT[i] == ch && idx-- == 0) return 1024 << i;
  return 0;
}
constexpr int toff(int t) { return (t & 1) * pat_stride('a', 0) + ((t >> 1) & 1) * pat_stride('b', 0) + (t >> 2) * pat_stride('c', 0); }
__device__ __forceinline__ int noff(int n) { return (n & 1) * pat_stride('p', 0) + ((n >> 1) & 1) * pat_stride('q', 0) + (n >> 2) * pat_stride('r', 0); }

constexpr int G_S0 = 145, G_PER = 215;
constexpr int G_D2 = 90;
constexpr int G_C = 65;
constexpr int G_TL = 92, G_T2 = 92;
constexpr int G_TW = -15;
constexpr int T_Q = 600, T_ML = 300;
constexpr int g_due(int hb) { return G_S0 + ((hb * G_PER) >> 1); }
constexpr u64 SPIN_CAP = 20000000ull;

constexpr int WS_EPOCH = 0;
constexpr int WS_ML = 4096;
constexpr int WS_QX = 32768;
constexpr int WS_P1 = 262144;
constexpr int WS_P2 = WS_P1 + (1 << 20);
constexpr int WS_END = WS_P2 + (1 << 20);

constexpr int PROW = 132, POFF = 544;
constexpr int WREG = POFF + 512;
constexpr int L_W = 0;
constexpr int L_ML = L_W + NW * WREG;
constexpr int L_F = L_ML + NW * 8;
constexpr int L_QF = L_F + NW * 4;
constexpr int L_CTL = L_QF + 256;
constexpr int L_FIN = L_CTL + 8;
constexpr int L_END = L_FIN + NW * 8;
constexpr int CW_NML = 0, CW_NLAST = 1, CW_QRDY = 2, CW_TG = 3, CW_T1 = 4, CW_T2 = 5;

__device__ __forceinline__ f32x4 mfma16(v4u a, v4u b, f32x4 c) {
  return __builtin_amdgcn_mfma_f32_16x16x32_bf16(__builtin_bit_cast(bf16x8, a), __builtin_bit_cast(bf16x8, b), c, 0, 0, 0);
}
__device__ __forceinline__ u32 pack_bf16(float lo, float hi) {
  bf16x2 r; r.x = (__bf16)lo; r.y = (__bf16)hi; return __builtin_bit_cast(u32, r);
}
__device__ __forceinline__ float dot2(u32 a, u32 b, float c) {
  return __builtin_amdgcn_fdot2_f32_bf16(__builtin_bit_cast(bf16x2, a), __builtin_bit_cast(bf16x2, b), c, false);
}
__device__ __forceinline__ u32 lo16s(u32 a, u32 b) { return __builtin_amdgcn_perm(b, a, 0x05040100u); }
__device__ __forceinline__ u32 hi16s(u32 a, u32 b) { return __builtin_amdgcn_perm(b, a, 0x07060302u); }
template <int CTRL> __device__ __forceinline__ float dppf(float x) {
  return __builtin_bit_cast(float, __builtin_amdgcn_mov_dpp(__builtin_bit_cast(int, x), CTRL, 0xf, 0xf, true));
}
template <int BANK> __device__ __forceinline__ float bank_put(float old, float src) {
  return __builtin_bit_cast(float, __builtin_amdgcn_update_dpp(__builtin_bit_cast(int, old), __builtin_bit_cast(int, src), 0xE4, 0xf, 1 << BANK, false));
}
constexpr int ROR4 = 0x124, ROR8 = 0x128, ROR12 = 0x12C, XOR1 = 0xB1, XOR2 = 0x4E;
__device__ __forceinline__ float xrow_max(float x) {
  auto s = __builtin_amdgcn_permlane16_swap(__float_as_uint(x), __float_as_uint(x), false, false);
  x = fmaxf(__uint_as_float(s[0]), __uint_as_float(s[1]));
  auto t = __builtin_amdgcn_permlane32_swap(__float_as_uint(x), __float_as_uint(x), false, false);
  return fmaxf(__uint_as_float(t[0]), __uint_as_float(t[1]));
}
__device__ __forceinline__ float xrow_sum(float x) {
  auto s = __builtin_amdgcn_permlane16_swap(__float_as_uint(x), __float_as_uint(x), false, false);
  x = __uint_as_float(s[0]) + __uint_as_float(s[1]);
  auto t = __builtin_amdgcn_permlane32_swap(__float_as_uint(x), __float_as_uint(x), false, false);
  return __uint_as_float(t[0]) + __uint_as_float(t[1]);
}
__device__ __forceinline__ float e2(float x) { return __builtin_amdgcn_exp2f(x); }
__device__ __forceinline__ u64 now() { return __builtin_amdgcn_s_memrealtime(); }
__device__ __forceinline__ void wg_barrier() {
  asm volatile("s_waitcnt lgkmcnt(0)" ::: "memory");
  __builtin_amdgcn_s_barrier();
  asm volatile("" ::: "memory");
}
#define FENCE() __builtin_amdgcn_sched_barrier(0)
__device__ __forceinline__ u32 cw_load(float* lds, int i) { return __hip_atomic_load((u32*)&lds[L_CTL + i], __ATOMIC_RELAXED, __HIP_MEMORY_SCOPE_WORKGROUP); }
__device__ __forceinline__ void cw_store(float* lds, int i, u32 v) { __hip_atomic_store((u32*)&lds[L_CTL + i], v, __ATOMIC_RELAXED, __HIP_MEMORY_SCOPE_WORKGROUP); }
__device__ __forceinline__ void cw_inc(float* lds, int i) { __hip_atomic_fetch_add((u32*)&lds[L_CTL + i], 1u, __ATOMIC_RELAXED, __HIP_MEMORY_SCOPE_WORKGROUP); }
template <int SLEEP> __device__ __forceinline__ void cw_wait(float* lds, int i, u32 want) {
  const u64 tb = now();
  while (cw_load(lds, i) < want) {
    if (now() - tb > 2 * SPIN_CAP) break;
    __builtin_amdgcn_s_sleep(SLEEP);
  }
  asm volatile("" ::: "memory");
}

__device__ __forceinline__ v4u rec_make(u32 a, u32 b, u32 t1, u32 t2) { return v4u{a, t1 ^ b, b, t2 ^ a}; }
__device__ __forceinline__ u32 rec_bad(v4u r, u32 t1, u32 t2) { return (r.y ^ r.z ^ t1) | (r.w ^ r.x ^ t2); }
constexpr int AUX_NT = 2, AUX_SC1 = 16;
__device__ __forceinline__ v4u ld_sc1(__amdgpu_buffer_rsrc_t rs, int off) { return __builtin_amdgcn_raw_buffer_load_b128(rs, off, 0, AUX_SC1); }
__device__ __forceinline__ void st_sc1(__amdgpu_buffer_rsrc_t rs, int off, v4u v) { __builtin_amdgcn_raw_buffer_store_b128(v, rs, off, 0, AUX_SC1); }
template <int N, int STRIDE, class F> __device__ __forceinline__ f32x2 gather_sum(__amdgpu_buffer_rsrc_t rs, int off, u32 t1, u32 t2, F mid) {
  static_assert(N == 2 || N == 4, "");
  v4u r[N];
  asm volatile("" ::: "memory");
#pragma unroll
  for (int i = 0; i < N; ++i) r[i] = ld_sc1(rs, off + i * STRIDE);
  FENCE();
  mid();
  FENCE();
  const u64 tb = now();
  for (;;) {
    u32 bad = 0u;
#pragma unroll
    for (int i = 0; i < N; ++i) bad |= rec_bad(r[i], t1, t2);
    if (__all(bad == 0u)) break;
    if (now() - tb > SPIN_CAP) break;
    asm volatile("" ::: "memory");
    if (bad != 0u) {
#pragma unroll
      for (int i = 0; i < N; ++i) r[i] = ld_sc1(rs, off + i * STRIDE);
    }
  }
  float a = __uint_as_float(r[0].x) + __uint_as_float(r[1].x), b = __uint_as_float(r[0].z) + __uint_as_float(r[1].z);
  if constexpr (N == 4) { a += __uint_as_float(r[2].x) + __uint_as_float(r[3].x); b += __uint_as_float(r[2].z) + __uint_as_float(r[3].z); }
  else { a += dppf<ROR4>(a); b += dppf<ROR4>(b); }
  a += dppf<ROR8>(a); b += dppf<ROR8>(b);
  return f32x2{xrow_sum(a), xrow_sum(b)};
}
__device__ __forceinline__ void wait_until(u32 base, int ticks) {
  for (int i = 0; i < 256 && (int)((u32)now() - base) < ticks; ++i) __builtin_amdgcn_s_sleep(1);
}

__global__ void __launch_bounds__(NTHR, 3) gqa_decode_kernel(const u16* __restrict__ x, const u16* __restrict__ kc, const u16* __restrict__ vc,
                                                             const u16* __restrict__ wq, const u16* __restrict__ wo, u16* __restrict__ out,
                                                             unsigned char* ws) {
  __shared__ __attribute__((aligned(16))) float lds[L_END];
  const int tid = threadIdx.x, lane = tid & 63, wv = __builtin_amdgcn_readfirstlane(tid >> 6);
  const int bid = blockIdx.x, g = bid >> 5, j = bid & 31;
  const int w = wv - 1, g4 = lane >> 4;
  int kl = ((lane >> 2) & 3) * 256 + g4 * 64 + (lane & 3) * 16;
  int sb = g * (SK * DH * 2) + (j & 1) * pat_stride('x', 0) + ((j >> 1) & 1) * pat_stride('x', 1) + ((j >> 2) & 1) * pat_stride('x', 2) + ((j >> 3) & 1) * pat_stride('y', 0) +
           (j >> 4) * pat_stride('y', 1) + (w & 1) * pat_stride('w', 0) + ((w >> 1) & 1) * pat_stride('w', 1) + ((w >> 2) & 1) * pat_stride('w', 2);
  asm volatile("" : "+v"(kl));
  asm volatile("" : "+s"(sb));
  asm volatile("" :: "s"(x), "s"(kc), "s"(vc), "s"(wq), "s"(wo), "s"(out), "s"(ws));
  const bool is_fin = (bid & 3) == ((bid >> 3) & 3);
  const int fin = bid >> 2;
  const __amdgpu_buffer_rsrc_t rs = __builtin_amdgcn_make_buffer_rsrc((void*)ws, 0, WS_END, 0x00020000);

  if (wv > 0) {
    const __amdgpu_buffer_rsrc_t rk = __builtin_amdgcn_make_buffer_rsrc((void*)kc, 0, 8 * SK * DH * 2, 0x00020000);
    const __amdgpu_buffer_rsrc_t rv = __builtin_amdgcn_make_buffer_rsrc((void*)vc, 0, 8 * SK * DH * 2, 0x00020000);
    const int vl = lane * 16;
#define LDK(n, t) __builtin_amdgcn_raw_buffer_load_b128(rk, kl, sb + noff(n) + toff(t), AUX_NT)
#define LDV(n, t) __builtin_amdgcn_raw_buffer_load_b128(rv, vl, sb + noff(n) + toff(t), AUX_NT)
    u32 tg = (u32)now();
    int lead = 0;
    const int gd2 = G_D2;
#define PACE(blk, t) do {                                                                                                                  \
      if (((t) & 3) == 0) { lead = g_due(2 * (blk) + ((t) >> 2)) - (int)((u32)now() - tg); FENCE(); }                                      \
      if ((blk) >= 3)                                                                                                                      \
        asm volatile("s_waitcnt vmcnt(3)\n\ts_cmp_gt_i32 %0, 0\n\ts_cbranch_scc0 1f\n\ts_waitcnt vmcnt(2)\n\ts_cmp_gt_i32 %0, %1\n\t"      \
                     "s_cbranch_scc0 1f\n\ts_waitcnt vmcnt(1)\n1:" :: "s"(lead), "s"(gd2) : "memory", "scc");                              \
      else                                                                                                                                 \
        asm volatile("s_waitcnt vmcnt(3)\n\ts_cmp_gt_i32 %0, 0\n\ts_cbranch_scc0 1f\n\ts_waitcnt vmcnt(2)\n1:" :: "s"(lead) : "memory", "scc"); \
      FENCE(); } while (0)
    v4u A[8], B[8], C[8], D[8];
    float* const wreg = &lds[L_W + w * WREG];
#pragma unroll
    for (int t = 0; t < 8; ++t) {
      if (t >= 4) { asm volatile("s_waitcnt vmcnt(3)" ::: "memory"); FENCE(); }
      A[t] = LDK(0, t); FENCE();
      if (t == 3) { wg_barrier(); FENCE(); }
    }
#pragma unroll
    for (int t = 0; t < 8; ++t) { PACE(1, t); B[t] = LDK(1, t); FENCE(); }
#pragma unroll
    for (int t = 0; t < 8; ++t) { PACE(2, t); C[t] = LDK(2, t); FENCE(); }
#pragma unroll
    for (int t = 0; t < 8; ++t) { PACE(3, t); D[t] = LDK(3, t); FENCE(); }
    cw_wait<1>(lds, CW_QRDY, 1u);
    const v4u qf = *(const v4u*)&lds[L_QF + (lane & 3) * 64 + g4 * 16 + ((lane >> 2) & 3) * 4];
    float sc[NBLK][2];

#define QKH(KK, n, G) do {                                                                                  \
      f32x4 d[4];                                                                                           \
      _Pragma("unroll") for (int t = 0; t < 4; ++t) d[t] = mfma16(KK[4 * (G) + t], qf, f32x4{0.f, 0.f, 0.f, 0.f}); \
      float T[4];                                                                                           \
      _Pragma("unroll") for (int r = 0; r < 4; ++r) {                                                       \
        float tt = d[r][0];                                                                                 \
        tt = bank_put<1>(tt, d[(1 + r) & 3][1]);                                                            \
        tt = bank_put<2>(tt, d[(2 + r) & 3][2]);                                                            \
        tt = bank_put<3>(tt, d[(3 + r) & 3][3]);                                                            \
        T[r] = tt; }                                                                                        \
      sc[n][G] = (T[0] + dppf<ROR4>(T[1])) + (dppf<ROR8>(T[2]) + dppf<ROR12>(T[3]));                        \
      asm volatile("" : "+v"(sc[n][G]));                                                                    \
      FENCE();                                                                                              \
    } while (0)
#define PV(VV, n) do {                                                                                      \
      const v4u P = *(const v4u*)(wreg + POFF + (n) * 64 + g4 * 16 + (lane & 3) * 4);                       \
      _Pragma("unroll") for (int e = 0; e < 4; ++e) {                                                       \
        const v4u lo = {lo16s(VV[0][e], VV[4][e]), lo16s(VV[1][e], VV[5][e]), lo16s(VV[2][e], VV[6][e]), lo16s(VV[3][e], VV[7][e])}; \
        const v4u hi = {hi16s(VV[0][e], VV[4][e]), hi16s(VV[1][e], VV[5][e]), hi16s(VV[2][e], VV[6][e]), hi16s(VV[3][e], VV[7][e])}; \
        acc[2 * e] = mfma16(lo, P, acc[2 * e]);                                                             \
        acc[2 * e + 1] = mfma16(hi, P, acc[2 * e + 1]); }                                                   \
      FENCE();                                                                                              \
    } while (0)
#define STEP(X, nx, Y, ny) _Pragma("unroll") for (int t = 0; t < 8; ++t) { PACE(nx, t); X[t] = LDK(nx, t); FENCE(); if (t == 2) QKH(Y, ny, 0); if (t == 6) QKH(Y, ny, 1); }

    QKH(A, 0, 0);
#pragma unroll
    for (int t = 0; t < 8; ++t) {
      PACE(4, t); A[t] = LDK(4, t); FENCE();
      if (t == 0) QKH(A, 0, 1);
      if (t == 1) QKH(B, 1, 0);
      if (t == 2) QKH(B, 1, 1);
      if (t == 3) QKH(C, 2, 0);
      if (t == 4) QKH(C, 2, 1);
      if (t == 5) QKH(D, 3, 0);
      if (t == 6) QKH(D, 3, 1);
    }
    STEP(B, 5, A, 4)
    STEP(A, 6, B, 5)
    STEP(B, 7, A, 6)
#pragma unroll
    for (int t = 0; t < 8; ++t) {
      PACE(8, t); A[t] = LDV(0, t); FENCE();
      if (t == 2) QKH(B, 7, 0);
      if (t == 4) QKH(B, 7, 1);
      if (t == 5) {
        float mx = fmaxf(sc[0][0], sc[0][1]);
#pragma unroll
        for (int n = 1; n < NBLK; ++n) mx = fmaxf(mx, fmaxf(sc[n][0], sc[n][1]));
        mx = fmaxf(mx, dppf<ROR4>(mx)); mx = fmaxf(mx, dppf<ROR8>(mx)); mx = xrow_max(mx);
        const float m = mx * C2;
        float l = 0.f;
#pragma unroll
        for (int n = 0; n < NBLK; ++n) {
          const float p0 = e2(fmaf(sc[n][0], C2, -m)), p1 = e2(fmaf(sc[n][1], C2, -m));
          l += p0 + p1;
          *(u32*)(wreg + POFF + n * 64 + g4 * 16 + (lane & 3) * 4 + ((lane >> 2) & 3)) = pack_bf16(p0, p1);
        }
        l += dppf<ROR4>(l); l += dppf<ROR8>(l); l = xrow_sum(l);
        if (lane < 4) *(f32x2*)&lds[L_ML + (lane * NW + w) * 2] = f32x2{m, l};
        asm volatile("s_waitcnt lgkmcnt(0)" ::: "memory");
        if (lane == 0) cw_inc(lds, CW_NML);
        FENCE();
      }
    }
    f32x4 acc[8];
#pragma unroll
    for (int i = 0; i < 8; ++i) acc[i] = f32x4{0.f, 0.f, 0.f, 0.f};
#pragma unroll 1
    for (int n = 1; n < NBLK - 1; n += 2) {
#pragma unroll
      for (int t = 0; t < 8; ++t) {
        PACE(8 + n, t); B[t] = LDV(n, t); FENCE();
        if (t == 6) { tg = __builtin_amdgcn_readfirstlane(cw_load(lds, CW_TG)); PV(A, n - 1); }
      }
#pragma unroll
      for (int t = 0; t < 8; ++t) {
        PACE(9 + n, t); A[t] = LDV(n + 1, t); FENCE();
        if (t == 6) { tg = __builtin_amdgcn_readfirstlane(cw_load(lds, CW_TG)); PV(B, n); }
      }
    }
#pragma unroll
    for (int t = 0; t < 8; ++t) {
      PACE(15, t); B[t] = LDV(NBLK - 1, t); FENCE();
      if (t == 6) PV(A, NBLK - 2);
    }
    if (lane == 0) cw_inc(lds, CW_NLAST);
    FENCE();
    const u32 t1 = cw_load(lds, CW_T1), t2 = cw_load(lds, CW_T2);
    int p1off = WS_P1 + ((g * 32 + (w & 3) * 8 + g4 * 2 + (lane & 1)) * 32 + j) * 128 + ((lane >> 1) & 7) * 16;
    LAS f32x4* rowp = (LAS f32x4*)(wreg + (lane & 3) * PROW + 32 * g4);
    const LAS f32x2* rdp = (const LAS f32x2*)&lds[L_W + (w & 3) * PROW + 2 * lane];
    const LAS f32x4* fp = (const LAS f32x4*)&lds[L_F + (w & 3) * NW];
    {
      v4u P = *(const v4u*)(wreg + POFF + (NBLK - 1) * 64 + g4 * 16 + (lane & 3) * 4);
      asm volatile("" : "+v"(P), "+v"(p1off), "+v"(rowp), "+v"(rdp), "+v"(fp));
      v4u fr[8];
#pragma unroll
      for (int u = 0; u < 3; ++u) {
#pragma unroll
        for (int e = 0; e < 4; ++e) { fr[2 * e][u] = lo16s(B[u][e], B[u + 4][e]); fr[2 * e + 1][u] = hi16s(B[u][e], B[u + 4][e]); }
        asm volatile("" : "+v"(fr[0][u]), "+v"(fr[1][u]), "+v"(fr[2][u]), "+v"(fr[3][u]), "+v"(fr[4][u]), "+v"(fr[5][u]), "+v"(fr[6][u]), "+v"(fr[7][u]));
        FENCE();
      }
#pragma unroll
      for (int e = 0; e < 4; ++e) { fr[2 * e][3] = lo16s(B[3][e], B[7][e]); fr[2 * e + 1][3] = hi16s(B[3][e], B[7][e]); }
#pragma unroll
      for (int i = 0; i < 8; ++i) acc[i] = mfma16(fr[i], P, acc[i]);
      FENCE();
#pragma unroll
      for (int i = 0; i < 8; ++i) rowp[i] = acc[i];
    }
    wg_barrier();
    if (w < 4) {
      f32x2 pv[NW];
#pragma unroll
      for (int i = 0; i < NW; ++i) pv[i] = rdp[i * (WREG / 2)];
      const f32x4 fa = fp[0], fb = fp[1];
      const f32x2 o = ((pv[0] * fa[0] + pv[1] * fa[1]) + (pv[2] * fa[2] + pv[3] * fa[3])) + ((pv[4] * fb[0] + pv[5] * fb[1]) + (pv[6] * fb[2] + pv[7] * fb[3]));
      st_sc1(rs, p1off, rec_make(__float_as_uint(o.x), __float_as_uint(o.y), t1, t2));
    }
    if (is_fin) {
      int off = WS_P2 + fin * 16384 + w * 2048 + lane * 16;
      asm volatile("" : "+v"(off));
      wg_barrier();
      __builtin_amdgcn_s_sleep(4);
      wait_until(tg, g_due(32) + G_TL + G_T2);
      const f32x2 s = gather_sum<2, 1024>(rs, off, t1, t2, [] {});
      if (lane < 4) *(f32x2*)&lds[L_FIN + w * 8 + lane * 2] = s;
      wg_barrier();
    }
  } else {
    __builtin_amdgcn_s_setprio(3);
    const u64 t0 = now();
    if (lane == 0) *(v4u*)&lds[L_CTL] = v4u{0u, 0u, 0u, (u32)t0};
    wg_barrier();
    FENCE();
    v4u wr[16], xr[2];
    {
      const __amdgpu_buffer_rsrc_t rq = __builtin_amdgcn_make_buffer_rsrc((void*)wq, 0, DM * HQ * DH * 2, 0x00020000);
      const int row0 = (j & 3) * 128 + (lane >> 3) * 16;
      const int vo = row0 * (HQ * DH * 2) + g * 1024 + (j >> 2) * 128 + (lane & 7) * 16;
#pragma unroll
      for (int r = 0; r < 16; ++r) wr[r] = __builtin_amdgcn_raw_buffer_load_b128(rq, vo, r * (HQ * DH * 2), AUX_NT);
      xr[0] = *(const GAS v4u*)(x + row0); xr[1] = *(const GAS v4u*)(x + row0 + 8);
    }
    const u64 ep = __hip_atomic_load((GAS u64*)(ws + WS_EPOCH), __ATOMIC_RELAXED, __HIP_MEMORY_SCOPE_AGENT);
    FENCE();
    float qv[8];
#pragma unroll
    for (int c = 0; c < 8; ++c) qv[c] = 0.f;
#pragma unroll
    for (int r = 0; r < 8; ++r)
#pragma unroll
      for (int e = 0; e < 4; ++e) {
        qv[2 * e] = dot2(lo16s(wr[2 * r][e], wr[2 * r + 1][e]), xr[r >> 2][r & 3], qv[2 * e]);
        qv[2 * e + 1] = dot2(hi16s(wr[2 * r][e], wr[2 * r + 1][e]), xr[r >> 2][r & 3], qv[2 * e + 1]);
      }
    float q4[4];
#pragma unroll
    for (int c = 0; c < 4; ++c) {
      const float a = qv[c] + dppf<ROR8>(qv[c]), b = qv[c + 4] + dppf<ROR8>(qv[c + 4]);
      q4[c] = xrow_sum((lane & 8) ? b : a);
    }
    u64 z = ep + 0x9E3779B97F4A7C15ull;
    z = (z ^ (z >> 30)) * 0xBF58476D1CE4E5B9ull; z = (z ^ (z >> 27)) * 0x94D049BB133111EBull; z ^= z >> 31;
    const u32 t1 = __builtin_amdgcn_readfirstlane((u32)z) | 1u, t2 = __builtin_amdgcn_readfirstlane((u32)(z >> 32));
    if (lane < 32)
      st_sc1(rs, WS_QX + ((g * 4 + (j & 3)) * 256 + (j >> 2) * 32 + (lane & 7) * 4 + ((lane >> 3) & 1) * 2 + (lane >> 4)) * 16,
             rec_make(__float_as_uint((lane & 16) ? q4[2] : q4[0]), __float_as_uint((lane & 16) ? q4[3] : q4[1]), t1, t2));
    if (lane == 0) { cw_store(lds, CW_T1, t1); cw_store(lds, CW_T2, t2); }
    {
      while (now() - t0 < T_Q) __builtin_amdgcn_s_sleep(2);
      const int off = WS_QX + (g * 1024 + lane) * 16;
      for (;;) {
        asm volatile("" ::: "memory");
#pragma unroll
        for (int i = 0; i < 16; ++i) wr[i] = __builtin_amdgcn_raw_buffer_load_b128(rs, off + (i & 3) * 1024, (i >> 2) * 4096, AUX_SC1);
        u32 bad = 0u;
#pragma unroll
        for (int i = 0; i < 16; ++i) bad |= rec_bad(wr[i], t1, t2);
        if (__all(bad == 0u)) break;
        if (now() - t0 > SPIN_CAP) break;
        __builtin_amdgcn_s_sleep(8);
      }
#pragma unroll
      for (int i = 0; i < 4; ++i) {
        const float a = (__uint_as_float(wr[i].x) + __uint_as_float(wr[4 + i].x)) + (__uint_as_float(wr[8 + i].x) + __uint_as_float(wr[12 + i].x));
        const float b = (__uint_as_float(wr[i].z) + __uint_as_float(wr[4 + i].z)) + (__uint_as_float(wr[8 + i].z) + __uint_as_float(wr[12 + i].z));
        *(u32*)&lds[L_QF + i * 64 + lane] = pack_bf16(a, b);
      }
    }
    asm volatile("s_waitcnt lgkmcnt(0)" ::: "memory");
    if (lane == 0) cw_store(lds, CW_QRDY, 1u);
    u32 tre;
    {
      cw_wait<8>(lds, CW_NML, NW);
      const f32x2 ml = *(const f32x2*)&lds[L_ML + ((lane >> 4) * NW + (lane & 7)) * 2];
      float M = ml.x;
      M = fmaxf(M, dppf<XOR1>(M)); M = fmaxf(M, dppf<XOR2>(M)); M = fmaxf(M, dppf<ROR4>(M));
      float L = ml.y * e2(ml.x - M);
      L += dppf<XOR1>(L); L += dppf<XOR2>(L); L += dppf<ROR4>(L);
      if ((lane & 15) == 0) st_sc1(rs, WS_ML + ((g * 5 + (lane >> 4)) * 32 + j) * 16, rec_make(__float_as_uint(M), __float_as_uint(L), t1, t2));
      const u64 tm = now();
      if (lane == 1) st_sc1(rs, WS_ML + ((g * 5 + 4) * 32 + j) * 16, rec_make((u32)tm, 0u, t1, t2));
      while (now() - tm < T_ML) __builtin_amdgcn_s_sleep(8);
      const int off = WS_ML + ((g * 5 + (lane >> 4)) * 32 + (lane & 15)) * 16;
      v4u r0, r1, r2;
      for (;;) {
        asm volatile("" ::: "memory");
        r2 = ld_sc1(rs, WS_ML + ((g * 5 + 4) * 32 + (lane & 31)) * 16);
        r0 = ld_sc1(rs, off); r1 = ld_sc1(rs, off + 256);
        if (__all((rec_bad(r0, t1, t2) | rec_bad(r1, t1, t2) | rec_bad(r2, t1, t2)) == 0u)) break;
        if (now() - tm > SPIN_CAP) break;
        __builtin_amdgcn_s_sleep(32);
      }
      float dt = (float)(int)(r2.x - (u32)tm);
      dt += dppf<XOR1>(dt); dt += dppf<XOR2>(dt); dt += dppf<ROR4>(dt); dt += dppf<ROR8>(dt); dt = xrow_sum(dt);
      tre = __builtin_amdgcn_readfirstlane((u32)tm + (u32)((int)(dt * (1.0f / 64.0f)) + G_C - g_due(18)));
      if (lane == 0) cw_store(lds, CW_TG, tre);
      float Mg = fmaxf(__uint_as_float(r0.x), __uint_as_float(r1.x));
      Mg = fmaxf(Mg, dppf<XOR1>(Mg)); Mg = fmaxf(Mg, dppf<XOR2>(Mg)); Mg = fmaxf(Mg, dppf<ROR4>(Mg)); Mg = fmaxf(Mg, dppf<ROR8>(Mg));
      float Lg = __uint_as_float(r0.z) * e2(__uint_as_float(r0.x) - Mg) + __uint_as_float(r1.z) * e2(__uint_as_float(r1.x) - Mg);
      Lg += dppf<XOR1>(Lg); Lg += dppf<XOR2>(Lg); Lg += dppf<ROR4>(Lg); Lg += dppf<ROR8>(Lg);
      if ((lane & 8) == 0) lds[L_F + (lane >> 4) * NW + (lane & 7)] = e2(ml.x - Mg) / Lg;
    }
    cw_wait<2>(lds, CW_NLAST, NW);
    wait_until(tre, g_due(32) + G_TW);
    {
      const __amdgpu_buffer_rsrc_t ro = __builtin_amdgcn_make_buffer_rsrc((void*)(wo + (size_t)(g * 512 + j * 16) * DM), 0, 16 * DM * 2, 0x00020000);
#pragma unroll
      for (int i = 0; i < 16; ++i) { const int r = (i >> 1) + (i & 1) * 8; wr[r] = __builtin_amdgcn_raw_buffer_load_b128(ro, lane * 16 + (r & 3) * 1024, (r >> 2) * 4096, AUX_NT); FENCE(); }
      FENCE();
    }
    wg_barrier();
    const u32 tb1 = (u32)now();
    int offv = WS_P1 + ((g * 32 + j) * 32 + (lane >> 3) * 4) * 128 + (lane & 7) * 16;
    int p2off = WS_P2 + ((lane >> 2) * 256 + bid) * 64 + (lane & 3) * 16;
    asm volatile("" : "+v"(offv), "+v"(p2off));
    wait_until(tb1, 40);
    wait_until(tre, g_due(32) + G_TL);
    u32 wp[8][8];
    const f32x2 o = gather_sum<4, 128>(rs, offv, t1, t2, [&] {
#pragma unroll
      for (int r = 0; r < 8; ++r) {
        asm volatile("" : "+v"(wr[r]), "+v"(wr[r + 8]));
#pragma unroll
        for (int e = 0; e < 4; ++e) { wp[r][2 * e] = lo16s(wr[r][e], wr[r + 8][e]); wp[r][2 * e + 1] = hi16s(wr[r][e], wr[r + 8][e]); }
        asm volatile("" : "+v"(wp[r][0]), "+v"(wp[r][1]), "+v"(wp[r][2]), "+v"(wp[r][3]), "+v"(wp[r][4]), "+v"(wp[r][5]), "+v"(wp[r][6]), "+v"(wp[r][7]));
      }
    });
    const u32 opk = pack_bf16(o.x, o.y);
    u32 so[8];
#pragma unroll
    for (int r = 0; r < 8; ++r) so[r] = __builtin_amdgcn_readlane(opk, r);
    FENCE();
    float y[8];
#pragma unroll
    for (int c = 0; c < 8; ++c) y[c] = 0.f;
#pragma unroll
    for (int r = 0; r < 8; ++r)
#pragma unroll
      for (int c = 0; c < 8; ++c) y[c] = dot2(wp[r][c], so[r], y[c]);
#pragma unroll
    for (int i = 0; i < 4; ++i)
      __builtin_amdgcn_raw_buffer_store_b128(rec_make(__float_as_uint(y[2 * i]), __float_as_uint(y[2 * i + 1]), t1, t2), rs, p2off, i * (16 * 256 * 64), AUX_SC1);
    FENCE();
    if (is_fin) {
      wg_barrier();
      wg_barrier();
      if (lane < 4) {
        f32x2 v[NW];
#pragma unroll
        for (int i = 0; i < NW; ++i) v[i] = *(const f32x2*)&lds[L_FIN + i * 8 + lane * 2];
        const f32x2 s = ((v[0] + v[1]) + (v[2] + v[3])) + ((v[4] + v[5]) + (v[6] + v[7]));
        *(GAS u32*)(out + (fin & 15) * 32 + lane * 8 + (fin >> 4) * 2) = pack_bf16(s.x, s.y);
        if (bid == 0 && lane == 0) __hip_atomic_store((GAS u64*)(ws + WS_EPOCH), ep + 1ull, __ATOMIC_RELAXED, __HIP_MEMORY_SCOPE_AGENT);
      }
    }
  }
}

extern "C" void kernel_launch(void* const* d_in, const int* in_sizes, int n_in, void* d_out, int out_size, void* d_ws, size_t ws_size,
                              hipStream_t stream) {
  gqa_decode_kernel<<<NWG, NTHR, 0, stream>>>((const u16*)d_in[0], (const u16*)d_in[1], (const u16*)d_in[2], (const u16*)d_in[3],
                                              (const u16*)d_in[4], (u16*)d_out, (unsigned char*)d_ws);
}
```

```cpp
#include <hip/hip_runtime.h>
#include <hip/hip_bf16.h>
#include <stdint.h>

typedef unsigned short u16;
typedef unsigned int u32;
typedef unsigned long long u64;
typedef u32 v4u __attribute__((ext_vector_type(4)));
typedef float f32x4 __attribute__((ext_vector_type(4)));
typedef float f32x2 __attribute__((ext_vector_type(2)));
typedef __bf16 bf16x8 __attribute__((ext_vector_type(8)));
typedef __bf16 bf16x2 __attribute__((ext_vector_type(2)));
#define GAS __attribute__((address_space(1)))
#define LAS __attribute__((address_space(3)))

constexpr int DM = 512, HQ = 32, SK = 65536, DH = 128;
constexpr int NWG = 256, NW = 8, NTHR = (NW + 1) * 64;
constexpr int NBLK = SK / 32 / 32 / NW;
constexpr float C2 = 0.088388347648318440f * 1.4426950408889634f;

#define KV_PAT "axwwwyypxqrxcb"
constexpr int pat_stride(char ch, int idx) {
  for (int i = 0; i < 14; ++i) if (KV_PAT[i] == ch && idx-- == 0) return 1024 << i;
  return 0;
}
constexpr int toff(int t) { return (t & 1) * pat_stride('a', 0) + ((t >> 1) & 1) * pat_stride('b', 0) + (t >> 2) * pat_stride('c', 0); }
__device__ __forceinline__ int noff(int n) { return (n & 1) * pat_stride('p', 0) + ((n >> 1) & 1) * pat_stride('q', 0) + (n >> 2) * pat_stride('r', 0); }

constexpr int G_S0 = 105, G_PER = 215;
constexpr int G_D2 = 90;
constexpr int G_C = 75;
constexpr int G_TL = 124, G_T2 = 94;
constexpr int T_Q = 600, T_ML = 300;
constexpr int g_due(int hb) { return G_S0 + ((hb * G_PER) >> 1); }
constexpr u64 SPIN_CAP = 20000000ull;

constexpr int WS_EPOCH = 0;
constexpr int WS_ML = 4096;
constexpr int WS_QX = 32768;
constexpr int WS_P1 = 262144;
constexpr int WS_P2 = WS_P1 + (1 << 20);
constexpr int WS_END = WS_P2 + (1 << 20);

constexpr int PROW = 132, POFF = 544;
constexpr int WREG = POFF + 512;
constexpr int L_W = 0;
constexpr int L_ML = L_W + NW * WREG;
constexpr int L_F = L_ML + NW * 8;
constexpr int L_QF = L_F + NW * 4;
constexpr int L_CTL = L_QF + 256;
constexpr int L_FIN = L_CTL + 8;
constexpr int L_END = L_FIN + NW * 8;
constexpr int CW_NML = 0, CW_NLAST = 1, CW_QRDY = 2, CW_TG = 3, CW_T1 = 4, CW_T2 = 5;

__device__ __forceinline__ f32x4 mfma16(v4u a, v4u b, f32x4 c) {
  return __builtin_amdgcn_mfma_f32_16x16x32_bf16(__builtin_bit_cast(bf16x8, a), __builtin_bit_cast(bf16x8, b), c, 0, 0, 0);
}
__device__ __forceinline__ u32 pack_bf16(float lo, float hi) {
  bf16x2 r; r.x = (__bf16)lo; r.y = (__bf16)hi; return __builtin_bit_cast(u32, r);
}
__device__ __forceinline__ float dot2(u32 a, u32 b, float c) {
  return __builtin_amdgcn_fdot2_f32_bf16(__builtin_bit_cast(bf16x2, a), __builtin_bit_cast(bf16x2, b), c, false);
}
__device__ __forceinline__ u32 lo16s(u32 a, u32 b) { return __builtin_amdgcn_perm(b, a, 0x05040100u); }
__device__ __forceinline__ u32 hi16s(u32 a, u32 b) { return __builtin_amdgcn_perm(b, a, 0x07060302u); }
template <int CTRL> __device__ __forceinline__ float dppf(float x) {
  return __builtin_bit_cast(float, __builtin_amdgcn_mov_dpp(__builtin_bit_cast(int, x), CTRL, 0xf, 0xf, true));
}
template <int BANK> __device__ __forceinline__ float bank_put(float old, float src) {
  return __builtin_bit_cast(float, __builtin_amdgcn_update_dpp(__builtin_bit_cast(int, old), __builtin_bit_cast(int, src), 0xE4, 0xf, 1 << BANK, false));
}
constexpr int ROR4 = 0x124, ROR8 = 0x128, ROR12 = 0x12C, XOR1 = 0xB1, XOR2 = 0x4E;
__device__ __forceinline__ float xrow_max(float x) {
  auto s = __builtin_amdgcn_permlane16_swap(__float_as_uint(x), __float_as_uint(x), false, false);
  x = fmaxf(__uint_as_float(s[0]), __uint_as_float(s[1]));
  auto t = __builtin_amdgcn_permlane32_swap(__float_as_uint(x), __float_as_uint(x), false, false);
  return fmaxf(__uint_as_float(t[0]), __uint_as_float(t[1]));
}
__device__ __forceinline__ float xrow_sum(float x) {
  auto s = __builtin_amdgcn_permlane16_swap(__float_as_uint(x), __float_as_uint(x), false, false);
  x = __uint_as_float(s[0]) + __uint_as_float(s[1]);
  auto t = __builtin_amdgcn_permlane32_swap(__float_as_uint(x), __float_as_uint(x), false, false);
  return __uint_as_float(t[0]) + __uint_as_float(t[1]);
}
__device__ __forceinline__ float e2(float x) { return __builtin_amdgcn_exp2f(x); }
__device__ __forceinline__ u64 now() { return __builtin_amdgcn_s_memrealtime(); }
__device__ __forceinline__ void wg_barrier() {
  asm volatile("s_waitcnt lgkmcnt(0)" ::: "memory");
  __builtin_amdgcn_s_barrier();
  asm volatile("" ::: "memory");
}
#define FENCE() __builtin_amdgcn_sched_barrier(0)
__device__ __forceinline__ u32 cw_load(float* lds, int i) { return __hip_atomic_load((u32*)&lds[L_CTL + i], __ATOMIC_RELAXED, __HIP_MEMORY_SCOPE_WORKGROUP); }
__device__ __forceinline__ void cw_store(float* lds, int i, u32 v) { __hip_atomic_store((u32*)&lds[L_CTL + i], v, __ATOMIC_RELAXED, __HIP_MEMORY_SCOPE_WORKGROUP); }
__device__ __forceinline__ void cw_inc(float* lds, int i) { __hip_atomic_fetch_add((u32*)&lds[L_CTL + i], 1u, __ATOMIC_RELAXED, __HIP_MEMORY_SCOPE_WORKGROUP); }
template <int SLEEP> __device__ __forceinline__ void cw_wait(float* lds, int i, u32 want) {
  const u64 tb = now();
  while (cw_load(lds, i) < want) {
    if (now() - tb > 2 * SPIN_CAP) break;
    __builtin_amdgcn_s_sleep(SLEEP);
  }
  asm volatile("" ::: "memory");
}

__device__ __forceinline__ v4u rec_make(u32 a, u32 b, u32 t1, u32 t2) { return v4u{a, t1 ^ b, b, t2 ^ a}; }
__device__ __forceinline__ u32 rec_bad(v4u r, u32 t1, u32 t2) { return (r.y ^ r.z ^ t1) | (r.w ^ r.x ^ t2); }
constexpr int AUX_NT = 2, AUX_SC1 = 16;
__device__ __forceinline__ v4u ld_sc1(__amdgpu_buffer_rsrc_t rs, int off) { return __builtin_amdgcn_raw_buffer_load_b128(rs, off, 0, AUX_SC1); }
__device__ __forceinline__ void st_sc1(__amdgpu_buffer_rsrc_t rs, int off, v4u v) { __builtin_amdgcn_raw_buffer_store_b128(v, rs, off, 0, AUX_SC1); }
template <int N, int STRIDE> __device__ __forceinline__ f32x2 gather_sum(__amdgpu_buffer_rsrc_t rs, int off, u32 t1, u32 t2) {
  static_assert(N == 2 || N == 4, "");
  const u64 tb = now();
  v4u r[N];
  bool ok = false;
  for (;;) {
    asm volatile("" ::: "memory");
    if (!ok) {
#pragma unroll
      for (int i = 0; i < N; ++i) r[i] = ld_sc1(rs, off + i * STRIDE);
      u32 bad = 0u;
#pragma unroll
      for (int i = 0; i < N; ++i) bad |= rec_bad(r[i], t1, t2);
      ok = bad == 0u;
    }
    if (__all(ok)) break;
    if (now() - tb > SPIN_CAP) break;
  }
  float a = __uint_as_float(r[0].x) + __uint_as_float(r[1].x), b = __uint_as_float(r[0].z) + __uint_as_float(r[1].z);
  if constexpr (N == 4) { a += __uint_as_float(r[2].x) + __uint_as_float(r[3].x); b += __uint_as_float(r[2].z) + __uint_as_float(r[3].z); }
  else { a += dppf<ROR4>(a); b += dppf<ROR4>(b); }
  a += dppf<ROR8>(a); b += dppf<ROR8>(b);
  return f32x2{xrow_sum(a), xrow_sum(b)};
}
__device__ __forceinline__ void wait_until(u32 base, int ticks) {
  for (int i = 0; i < 256 && (int)((u32)now() - base) < ticks; ++i) __builtin_amdgcn_s_sleep(1);
}

__global__ void __launch_bounds__(NTHR, 3) gqa_decode_kernel(const u16* __restrict__ x, const u16* __restrict__ kc, const u16* __restrict__ vc,
                                                             const u16* __restrict__ wq, const u16* __restrict__ wo, u16* __restrict__ out,
                                                             unsigned char* ws) {
  __shared__ __attribute__((aligned(16))) float lds[L_END];
  const int tid = threadIdx.x, lane = tid & 63, wv = __builtin_amdgcn_readfirstlane(tid >> 6);
  const int bid = blockIdx.x, g = bid >> 5, j = bid & 31;
  const int w = wv - 1, g4 = lane >> 4;
  int kl = ((lane >> 2) & 3) * 256 + g4 * 64 + (lane & 3) * 16;
  int sb = g * (SK * DH * 2) + (j & 1) * pat_stride('x', 0) + ((j >> 1) & 1) * pat_stride('x', 1) + ((j >> 2) & 1) * pat_stride('x', 2) + ((j >> 3) & 1) * pat_stride('y', 0) +
           (j >> 4) * pat_stride('y', 1) + (w & 1) * pat_stride('w', 0) + ((w >> 1) & 1) * pat_stride('w', 1) + ((w >> 2) & 1) * pat_stride('w', 2);
  asm volatile("" : "+v"(kl));
  asm volatile("" : "+s"(sb));
  asm volatile("" :: "s"(x), "s"(kc), "s"(vc), "s"(wq), "s"(wo), "s"(out), "s"(ws));
  const bool is_fin = (bid & 3) == ((bid >> 3) & 3);
  const int fin = bid >> 2;
  const __amdgpu_buffer_rsrc_t rs = __builtin_amdgcn_make_buffer_rsrc((void*)ws, 0, WS_END, 0x00020000);

  if (wv > 0) {
    const __amdgpu_buffer_rsrc_t rk = __builtin_amdgcn_make_buffer_rsrc((void*)kc, 0, 8 * SK * DH * 2, 0x00020000);
    const __amdgpu_buffer_rsrc_t rv = __builtin_amdgcn_make_buffer_rsrc((void*)vc, 0, 8 * SK * DH * 2, 0x00020000);
    const int vl = lane * 16;
#define LDK(n, t) __builtin_amdgcn_raw_buffer_load_b128(rk, kl, sb + noff(n) + toff(t), AUX_NT)
#define LDV(n, t) __builtin_amdgcn_raw_buffer_load_b128(rv, vl, sb + noff(n) + toff(t), AUX_NT)
    u32 tg = (u32)now();
    int lead = 0;
    const int gd2 = G_D2;
#define PACE(blk, t) do {                                                                                                                  \
      if (((t) & 3) == 0) { lead = g_due(2 * (blk) + ((t) >> 2)) - (int)((u32)now() - tg); FENCE(); }                                      \
      if ((blk) >= 3)                                                                                                                      \
        asm volatile("s_waitcnt vmcnt(3)\n\ts_cmp_gt_i32 %0, 0\n\ts_cbranch_scc0 1f\n\ts_waitcnt vmcnt(2)\n\ts_cmp_gt_i32 %0, %1\n\t"      \
                     "s_cbranch_scc0 1f\n\ts_waitcnt vmcnt(1)\n1:" :: "s"(lead), "s"(gd2) : "memory", "scc");                              \
      else                                                                                                                                 \
        asm volatile("s_waitcnt vmcnt(3)\n\ts_cmp_gt_i32 %0, 0\n\ts_cbranch_scc0 1f\n\ts_waitcnt vmcnt(2)\n1:" :: "s"(lead) : "memory", "scc"); \
      FENCE(); } while (0)
    v4u A[8], B[8], C[8], D[8];
    float* const wreg = &lds[L_W + w * WREG];
#pragma unroll
    for (int t = 0; t < 8; ++t) {
      if (t >= 4) { asm volatile("s_waitcnt vmcnt(3)" ::: "memory"); FENCE(); }
      A[t] = LDK(0, t); FENCE();
      if (t == 3) { wg_barrier(); FENCE(); }
    }
#pragma unroll
    for (int t = 0; t < 8; ++t) { PACE(1, t); B[t] = LDK(1, t); FENCE(); }
#pragma unroll
    for (int t = 0; t < 8; ++t) { PACE(2, t); C[t] = LDK(2, t); FENCE(); }
#pragma unroll
    for (int t = 0; t < 8; ++t) { PACE(3, t); D[t] = LDK(3, t); FENCE(); }
    cw_wait<1>(lds, CW_QRDY, 1u);
    const v4u qf = *(const v4u*)&lds[L_QF + (lane & 3) * 64 + g4 * 16 + ((lane >> 2) & 3) * 4];
    float sc[NBLK][2];

#define QKH(KK, n, G) do {                                                                                  \
      f32x4 d[4];                                                                                           \
      _Pragma("unroll") for (int t = 0; t < 4; ++t) d[t] = mfma16(KK[4 * (G) + t], qf, f32x4{0.f, 0.f, 0.f, 0.f}); \
      float T[4];                                                                                           \
      _Pragma("unroll") for (int r = 0; r < 4; ++r) {                                                       \
        float tt = d[r][0];                                                                                 \
        tt = bank_put<1>(tt, d[(1 + r) & 3][1]);                                                            \
        tt = bank_put<2>(tt, d[(2 + r) & 3][2]);                                                            \
        tt = bank_put<3>(tt, d[(3 + r) & 3][3]);                                                            \
        T[r] = tt; }                                                                                        \
      sc[n][G] = (T[0] + dppf<ROR4>(T[1])) + (dppf<ROR8>(T[2]) + dppf<ROR12>(T[3]));                        \
      asm volatile("" : "+v"(sc[n][G]));                                                                    \
      FENCE();                                                                                              \
    } while (0)
#define PV(VV, n) do {                                                                                      \
      const v4u P = *(const v4u*)(wreg + POFF + (n) * 64 + g4 * 16 + (lane & 3) * 4);                       \
      _Pragma("unroll") for (int e = 0; e < 4; ++e) {                                                       \
        const v4u lo = {lo16s(VV[0][e], VV[4][e]), lo16s(VV[1][e], VV[5][e]), lo16s(VV[2][e], VV[6][e]), lo16s(VV[3][e], VV[7][e])}; \
        const v4u hi = {hi16s(VV[0][e], VV[4][e]), hi16s(VV[1][e], VV[5][e]), hi16s(VV[2][e], VV[6][e]), hi16s(VV[3][e], VV[7][e])}; \
        acc[2 * e] = mfma16(lo, P, acc[2 * e]);                                                             \
        acc[2 * e + 1] = mfma16(hi, P, acc[2 * e + 1]); }                                                   \
      FENCE();                                                                                              \
    } while (0)
#define STEP(X, nx, Y, ny) _Pragma("unroll") for (int t = 0; t < 8; ++t) { PACE(nx, t); X[t] = LDK(nx, t); FENCE(); if (t == 2) QKH(Y, ny, 0); if (t == 6) QKH(Y, ny, 1); }

    QKH(A, 0, 0);
#pragma unroll
    for (int t = 0; t < 8; ++t) {
      PACE(4, t); A[t] = LDK(4, t); FENCE();
      if (t == 0) QKH(A, 0, 1);
      if (t == 1) QKH(B, 1, 0);
      if (t == 2) QKH(B, 1, 1);
      if (t == 3) QKH(C, 2, 0);
      if (t == 4) QKH(C, 2, 1);
      if (t == 5) QKH(D, 3, 0);
      if (t == 6) QKH(D, 3, 1);
    }
    STEP(B, 5, A, 4)
    STEP(A, 6, B, 5)
    STEP(B, 7, A, 6)
#pragma unroll
    for (int t = 0; t < 8; ++t) {
      PACE(8, t); A[t] = LDV(0, t); FENCE();
      if (t == 2) QKH(B, 7, 0);
      if (t == 4) QKH(B, 7, 1);
      if (t == 5) {
        float mx = fmaxf(sc[0][0], sc[0][1]);
#pragma unroll
        for (int n = 1; n < NBLK; ++n) mx = fmaxf(mx, fmaxf(sc[n][0], sc[n][1]));
        mx = fmaxf(mx, dppf<ROR4>(mx)); mx = fmaxf(mx, dppf<ROR8>(mx)); mx = xrow_max(mx);
        const float m = mx * C2;
        float l = 0.f;
#pragma unroll
        for (int n = 0; n < NBLK; ++n) {
          const float p0 = e2(fmaf(sc[n][0], C2, -m)), p1 = e2(fmaf(sc[n][1], C2, -m));
          l += p0 + p1;
          *(u32*)(wreg + POFF + n * 64 + g4 * 16 + (lane & 3) * 4 + ((lane >> 2) & 3)) = pack_bf16(p0, p1);
        }
        l += dppf<ROR4>(l); l += dppf<ROR8>(l); l = xrow_sum(l);
        if (lane < 4) *(f32x2*)&lds[L_ML + (lane * NW + w) * 2] = f32x2{m, l};
        asm volatile("s_waitcnt lgkmcnt(0)" ::: "memory");
        if (lane == 0) cw_inc(lds, CW_NML);
        FENCE();
      }
    }
    f32x4 acc[8];
#pragma unroll
    for (int i = 0; i < 8; ++i) acc[i] = f32x4{0.f, 0.f, 0.f, 0.f};
#pragma unroll 1
    for (int n = 1; n < NBLK - 1; n += 2) {
#pragma unroll
      for (int t = 0; t < 8; ++t) {
        PACE(8 + n, t); B[t] = LDV(n, t); FENCE();
        if (t == 6) { tg = __builtin_amdgcn_readfirstlane(cw_load(lds, CW_TG)); PV(A, n - 1); }
      }
#pragma unroll
      for (int t = 0; t < 8; ++t) {
        PACE(9 + n, t); A[t] = LDV(n + 1, t); FENCE();
        if (t == 6) { tg = __builtin_amdgcn_readfirstlane(cw_load(lds, CW_TG)); PV(B, n); }
      }
    }
#pragma unroll
    for (int t = 0; t < 8; ++t) {
      PACE(15, t); B[t] = LDV(NBLK - 1, t); FENCE();
      if (t == 6) PV(A, NBLK - 2);
    }
    if (lane == 0) cw_inc(lds, CW_NLAST);
    FENCE();
    const u32 t1 = cw_load(lds, CW_T1), t2 = cw_load(lds, CW_T2);
    int p1off = WS_P1 + ((g * 32 + (w & 3) * 8 + g4 * 2 + (lane & 1)) * 32 + j) * 128 + ((lane >> 1) & 7) * 16;
    LAS f32x4* rowp = (LAS f32x4*)(wreg + (lane & 3) * PROW + 32 * g4);
    const LAS f32x2* rdp = (const LAS f32x2*)&lds[L_W + (w & 3) * PROW + 2 * lane];
    const LAS f32x4* fp = (const LAS f32x4*)&lds[L_F + (w & 3) * NW];
    {
      v4u P = *(const v4u*)(wreg + POFF + (NBLK - 1) * 64 + g4 * 16 + (lane & 3) * 4);
      asm volatile("" : "+v"(P), "+v"(p1off), "+v"(rowp), "+v"(rdp), "+v"(fp));
      v4u fr[8];
#pragma unroll
      for (int u = 0; u < 3; ++u) {
#pragma unroll
        for (int e = 0; e < 4; ++e) { fr[2 * e][u] = lo16s(B[u][e], B[u + 4][e]); fr[2 * e + 1][u] = hi16s(B[u][e], B[u + 4][e]); }
        asm volatile("" : "+v"(fr[0][u]), "+v"(fr[1][u]), "+v"(fr[2][u]), "+v"(fr[3][u]), "+v"(fr[4][u]), "+v"(fr[5][u]), "+v"(fr[6][u]), "+v"(fr[7][u]));
        FENCE();
      }
#pragma unroll
      for (int e = 0; e < 4; ++e) { fr[2 * e][3] = lo16s(B[3][e], B[7][e]); fr[2 * e + 1][3] = hi16s(B[3][e], B[7][e]); }
#pragma unroll
      for (int i = 0; i < 8; ++i) acc[i] = mfma16(fr[i], P, acc[i]);
      FENCE();
#pragma unroll
      for (int i = 0; i < 8; ++i) rowp[i] = acc[i];
    }
    wg_barrier();
    if (w < 4) {
      f32x2 pv[NW];
#pragma unroll
      for (int i = 0; i < NW; ++i) pv[i] = rdp[i * (WREG / 2)];
      const f32x4 fa = fp[0], fb = fp[1];
      const f32x2 o = ((pv[0] * fa[0] + pv[1] * fa[1]) + (pv[2] * fa[2] + pv[3] * fa[3])) + ((pv[4] * fb[0] + pv[5] * fb[1]) + (pv[6] * fb[2] + pv[7] * fb[3]));
      st_sc1(rs, p1off, rec_make(__float_as_uint(o.x), __float_as_uint(o.y), t1, t2));
    }
    if (is_fin) {
      int off = WS_P2 + fin * 16384 + w * 2048 + lane * 16;
      asm volatile("" : "+v"(off));
      wg_barrier();
      __builtin_amdgcn_s_sleep(4);
      wait_until(tg, g_due(32) + G_TL + G_T2);
      const f32x2 s = gather_sum<2, 1024>(rs, off, t1, t2);
      if (lane < 4) *(f32x2*)&lds[L_FIN + w * 8 + lane * 2] = s;
      wg_barrier();
    }
  } else {
    __builtin_amdgcn_s_setprio(3);
    const u64 t0 = now();
    if (lane == 0) *(v4u*)&lds[L_CTL] = v4u{0u, 0u, 0u, (u32)t0};
    wg_barrier();
    FENCE();
    v4u wr[16], xr[2];
    {
      const __amdgpu_buffer_rsrc_t rq = __builtin_amdgcn_make_buffer_rsrc((void*)wq, 0, DM * HQ * DH * 2, 0x00020000);
      const int row0 = (j & 3) * 128 + (lane >> 3) * 16;
      const int vo = row0 * (HQ * DH * 2) + g * 1024 + (j >> 2) * 128 + (lane & 7) * 16;
#pragma unroll
      for (int r = 0; r < 16; ++r) wr[r] = __builtin_amdgcn_raw_buffer_load_b128(rq, vo, r * (HQ * DH * 2), AUX_NT);
      xr[0] = *(const GAS v4u*)(x + row0); xr[1] = *(const GAS v4u*)(x + row0 + 8);
    }
    const u64 ep = __hip_atomic_load((GAS u64*)(ws + WS_EPOCH), __ATOMIC_RELAXED, __HIP_MEMORY_SCOPE_AGENT);
    FENCE();
    float qv[8];
#pragma unroll
    for (int c = 0; c < 8; ++c) qv[c] = 0.f;
#pragma unroll
    for (int r = 0; r < 8; ++r)
#pragma unroll
      for (int e = 0; e < 4; ++e) {
        qv[2 * e] = dot2(lo16s(wr[2 * r][e], wr[2 * r + 1][e]), xr[r >> 2][r & 3], qv[2 * e]);
        qv[2 * e + 1] = dot2(hi16s(wr[2 * r][e], wr[2 * r + 1][e]), xr[r >> 2][r & 3], qv[2 * e + 1]);
      }
    float q4[4];
#pragma unroll
    for (int c = 0; c < 4; ++c) {
      const float a = qv[c] + dppf<ROR8>(qv[c]), b = qv[c + 4] + dppf<ROR8>(qv[c + 4]);
      q4[c] = xrow_sum((lane & 8) ? b : a);
    }
    u64 z = ep + 0x9E3779B97F4A7C15ull;
    z = (z ^ (z >> 30)) * 0xBF58476D1CE4E5B9ull; z = (z ^ (z >> 27)) * 0x94D049BB133111EBull; z ^= z >> 31;
    const u32 t1 = __builtin_amdgcn_readfirstlane((u32)z) | 1u, t2 = __builtin_amdgcn_readfirstlane((u32)(z >> 32));
    if (lane < 32)
      st_sc1(rs, WS_QX + ((g * 4 + (j & 3)) * 256 + (j >> 2) * 32 + (lane & 7) * 4 + ((lane >> 3) & 1) * 2 + (lane >> 4)) * 16,
             rec_make(__float_as_uint((lane & 16) ? q4[2] : q4[0]), __float_as_uint((lane & 16) ? q4[3] : q4[1]), t1, t2));
    if (lane == 0) { cw_store(lds, CW_T1, t1); cw_store(lds, CW_T2, t2); }
    {
      while (now() - t0 < T_Q) __builtin_amdgcn_s_sleep(2);
      const int off = WS_QX + (g * 1024 + lane) * 16;
      for (;;) {
        asm volatile("" ::: "memory");
#pragma unroll
        for (int i = 0; i < 16; ++i) wr[i] = __builtin_amdgcn_raw_buffer_load_b128(rs, off + (i & 3) * 1024, (i >> 2) * 4096, AUX_SC1);
        u32 bad = 0u;
#pragma unroll
        for (int i = 0; i < 16; ++i) bad |= rec_bad(wr[i], t1, t2);
        if (__all(bad == 0u)) break;
        if (now() - t0 > SPIN_CAP) break;
        __builtin_amdgcn_s_sleep(8);
      }
#pragma unroll
      for (int i = 0; i < 4; ++i) {
        const float a = (__uint_as_float(wr[i].x) + __uint_as_float(wr[4 + i].x)) + (__uint_as_float(wr[8 + i].x) + __uint_as_float(wr[12 + i].x));
        const float b = (__uint_as_float(wr[i].z) + __uint_as_float(wr[4 + i].z)) + (__uint_as_float(wr[8 + i].z) + __uint_as_float(wr[12 + i].z));
        *(u32*)&lds[L_QF + i * 64 + lane] = pack_bf16(a, b);
      }
    }
    asm volatile("s_waitcnt lgkmcnt(0)" ::: "memory");
    if (lane == 0) cw_store(lds, CW_QRDY, 1u);
    u32 tre;
    {
      cw_wait<8>(lds, CW_NML, NW);
      const f32x2 ml = *(const f32x2*)&lds[L_ML + ((lane >> 4) * NW + (lane & 7)) * 2];
      float M = ml.x;
      M = fmaxf(M, dppf<XOR1>(M)); M = fmaxf(M, dppf<XOR2>(M)); M = fmaxf(M, dppf<ROR4>(M));
      float L = ml.y * e2(ml.x - M);
      L += dppf<XOR1>(L); L += dppf<XOR2>(L); L += dppf<ROR4>(L);
      if ((lane & 15) == 0) st_sc1(rs, WS_ML + ((g * 5 + (lane >> 4)) * 32 + j) * 16, rec_make(__float_as_uint(M), __float_as_uint(L), t1, t2));
      const u64 tm = now();
      if (lane == 1) st_sc1(rs, WS_ML + ((g * 5 + 4) * 32 + j) * 16, rec_make((u32)tm, 0u, t1, t2));
      while (now() - tm < T_ML) __builtin_amdgcn_s_sleep(8);
      const int off = WS_ML + ((g * 5 + (lane >> 4)) * 32 + (lane & 15)) * 16;
      v4u r0, r1, r2;
      for (;;) {
        asm volatile("" ::: "memory");
        r2 = ld_sc1(rs, WS_ML + ((g * 5 + 4) * 32 + (lane & 31)) * 16);
        r0 = ld_sc1(rs, off); r1 = ld_sc1(rs, off + 256);
        if (__all((rec_bad(r0, t1, t2) | rec_bad(r1, t1, t2) | rec_bad(r2, t1, t2)) == 0u)) break;
        if (now() - tm > SPIN_CAP) break;
        __builtin_amdgcn_s_sleep(32);
      }
      float dt = (float)(int)(r2.x - (u32)tm);
      dt += dppf<XOR1>(dt); dt += dppf<XOR2>(dt); dt += dppf<ROR4>(dt); dt += dppf<ROR8>(dt); dt = xrow_sum(dt);
      tre = __builtin_amdgcn_readfirstlane((u32)tm + (u32)((int)(dt * (1.0f / 64.0f)) + G_C - g_due(18)));
      if (lane == 0) cw_store(lds, CW_TG, tre);
      float Mg = fmaxf(__uint_as_float(r0.x), __uint_as_float(r1.x));
      Mg = fmaxf(Mg, dppf<XOR1>(Mg)); Mg = fmaxf(Mg, dppf<XOR2>(Mg)); Mg = fmaxf(Mg, dppf<ROR4>(Mg)); Mg = fmaxf(Mg, dppf<ROR8>(Mg));
      float Lg = __uint_as_float(r0.z) * e2(__uint_as_float(r0.x) - Mg) + __uint_as_float(r1.z) * e2(__uint_as_float(r1.x) - Mg);
      Lg += dppf<XOR1>(Lg); Lg += dppf<XOR2>(Lg); Lg += dppf<ROR4>(Lg); Lg += dppf<ROR8>(Lg);
      if ((lane & 8) == 0) lds[L_F + (lane >> 4) * NW + (lane & 7)] = e2(ml.x - Mg) / Lg;
    }
    cw_wait<2>(lds, CW_NLAST, NW);
    {
      const __amdgpu_buffer_rsrc_t ro = __builtin_amdgcn_make_buffer_rsrc((void*)(wo + (size_t)(g * 512 + j * 16) * DM), 0, 16 * DM * 2, 0x00020000);
#pragma unroll
      for (int r = 0; r < 16; ++r) wr[r] = __builtin_amdgcn_raw_buffer_load_b128(ro, lane * 16 + (r & 3) * 1024, (r >> 2) * 4096, AUX_NT);
      FENCE();
    }
    wg_barrier();
    u32 wp[8][8];
#pragma unroll
    for (int r = 0; r < 8; ++r) {
#pragma unroll
      for (int e = 0; e < 4; ++e) { wp[r][2 * e] = lo16s(wr[r][e], wr[r + 8][e]); wp[r][2 * e + 1] = hi16s(wr[r][e], wr[r + 8][e]); }
      asm volatile("" : "+v"(wp[r][0]), "+v"(wp[r][1]), "+v"(wp[r][2]), "+v"(wp[r][3]), "+v"(wp[r][4]), "+v"(wp[r][5]), "+v"(wp[r][6]), "+v"(wp[r][7]));
    }
    FENCE();
    int offv = WS_P1 + ((g * 32 + j) * 32 + (lane >> 3) * 4) * 128 + (lane & 7) * 16;
    int p2off = WS_P2 + ((lane >> 2) * 256 + bid) * 64 + (lane & 3) * 16;
    asm volatile("" : "+v"(offv), "+v"(p2off));
    __builtin_amdgcn_s_sleep(10);
    wait_until(tre, g_due(32) + G_TL);
    const f32x2 o = gather_sum<4, 128>(rs, offv, t1, t2);
    const u32 opk = pack_bf16(o.x, o.y);
    u32 so[8];
#pragma unroll
    for (int r = 0; r < 8; ++r) so[r] = __builtin_amdgcn_readlane(opk, r);
    FENCE();
    float y[8];
#pragma unroll
    for (int c = 0; c < 8; ++c) y[c] = 0.f;
#pragma unroll
    for (int r = 0; r < 8; ++r)
#pragma unroll
      for (int c = 0; c < 8; ++c) y[c] = dot2(wp[r][c], so[r], y[c]);
#pragma unroll
    for (int i = 0; i < 4; ++i)
      __builtin_amdgcn_raw_buffer_store_b128(rec_make(__float_as_uint(y[2 * i]), __float_as_uint(y[2 * i + 1]), t1, t2), rs, p2off, i * (16 * 256 * 64), AUX_SC1);
    FENCE();
    if (is_fin) {
      wg_barrier();
      wg_barrier();
      if (lane < 4) {
        f32x2 v[NW];
#pragma unroll
        for (int i = 0; i < NW; ++i) v[i] = *(const f32x2*)&lds[L_FIN + i * 8 + lane * 2];
        const f32x2 s = ((v[0] + v[1]) + (v[2] + v[3])) + ((v[4] + v[5]) + (v[6] + v[7]));
        *(GAS u32*)(out + (fin & 15) * 32 + lane * 8 + (fin >> 4) * 2) = pack_bf16(s.x, s.y);
        if (bid == 0 && lane == 0) __hip_atomic_store((GAS u64*)(ws + WS_EPOCH), ep + 1ull, __ATOMIC_RELAXED, __HIP_MEMORY_SCOPE_AGENT);
      }
    }
  }
}

extern "C" void kernel_launch(void* const* d_in, const int* in_sizes, int n_in, void* d_out, int out_size, void* d_ws, size_t ws_size,
                              hipStream_t stream) {
  gqa_decode_kernel<<<NWG, NTHR, 0, stream>>>((const u16*)d_in[0], (const u16*)d_in[1], (const u16*)d_in[2], (const u16*)d_in[3],
                                              (const u16*)d_in[4], (u16*)d_out, (unsigned char*)d_ws);
}
```
